```python
import torch

def _softmax(x, axis=-1):
    return torch.softmax(x, dim=axis)
def _gelu(x):
    return torch.nn.functional.gelu(x, approximate="tanh")
def _silu(x):
    return torch.nn.functional.silu(x)
def _sigmoid(x):
    return torch.sigmoid(x)
def _layernorm(x, axis=-1, eps=1e-5):
    m = x.mean(dim=axis, keepdim=True)
    v = x.var(dim=axis, keepdim=True, unbiased=False)
    return (x - m) / torch.sqrt(v + eps)
def _scatter_add(tgt, idx, src, axis=0):
    return tgt.clone().index_add_(axis, idx, src)
def _segment_sum(src, seg, n):
    out = torch.zeros((n,) + src.shape[1:], dtype=src.dtype)
    return out.index_add_(0, seg, src)
def _pad(x, pw):
    flat = tuple(v for pair in reversed(pw) for v in pair)
    return torch.nn.functional.pad(x, flat)

kernel_name = 'gated_mix__mha_block__ssm_conv_s4__prompt'

def setup_inputs():

    _w = (lambda a: (lambda t: t.to(torch.bfloat16) if t.is_floating_point() else t)(torch.as_tensor(a)))
    def _A_setup():
        B, S, D, H = 1, 2048, 448, 16
        return {
            "x": _w((lambda *s: torch.randn(*s, dtype=torch.bfloat16) * 0.05)(B, S, D)),
            "wqkv": _w((lambda *s: torch.randn(*s, dtype=torch.bfloat16) * 0.8)(D, 3 * D)),
            "wo": _w((lambda *s: torch.randn(*s, dtype=torch.bfloat16) * 0.05)(D, D)),
        }
    _A_in = _A_setup()

    def _B_setup():
        B, L, D, N = 1, 2048, 448, 256
        A_re = -(lambda _x: torch.exp(torch.as_tensor(_x)))((lambda *s: torch.randn(*s, dtype=torch.bfloat16) * 6.4)(D, N))
        A_im = torch.pi * (lambda *s: torch.randn(*s, dtype=torch.bfloat16) * 0.1)(D, N)
        B_re = (lambda *s: torch.randn(*s, dtype=torch.bfloat16) * 0.2)(D, N)
        B_im = (lambda *s: torch.randn(*s, dtype=torch.bfloat16) * 0.2)(D, N)
        C_re = (lambda *s: torch.randn(*s, dtype=torch.bfloat16) * 0.2)(D, N)
        C_im = (lambda *s: torch.randn(*s, dtype=torch.bfloat16) * 0.2)(D, N)
        log_dt = (lambda *s: torch.randn(*s, dtype=torch.bfloat16) * 0.1)(D) * 0.5 - 4.0
        Dskip = (lambda *s: torch.randn(*s, dtype=torch.bfloat16) * 0.1)(D)
        return {
            "u": _w((lambda *s: torch.randn(*s, dtype=torch.bfloat16) * 0.05)(B, L, D)),
            "A_re": _w(A_re),
            "A_im": _w(A_im),
            "B_re": _w(B_re),
            "B_im": _w(B_im),
            "C_re": _w(C_re),
            "C_im": _w(C_im),
            "log_dt": _w(log_dt),
            "Dskip": _w(Dskip),
        }
    _B_in = _B_setup()

    return {
        "A_x": _A_in["x"],
        "A_wqkv": _A_in["wqkv"],
        "A_wo": _A_in["wo"],
        "B_u": _B_in["u"],
        "B_A_re": _B_in["A_re"],
        "B_A_im": _B_in["A_im"],
        "B_B_re": _B_in["B_re"],
        "B_B_im": _B_in["B_im"],
        "B_C_re": _B_in["C_re"],
        "B_C_im": _B_in["C_im"],
        "B_log_dt": _B_in["log_dt"],
        "B_Dskip": _B_in["Dskip"],
        "gate_w": _w((lambda *s: torch.randn(*s, dtype=torch.bfloat16) * 0.05)(448) * 16.0),
    }

def reference(A_x, A_wqkv, A_wo, B_u, B_A_re, B_A_im, B_B_re, B_B_im, B_C_re, B_C_im, B_log_dt, B_Dskip, gate_w):
    def _A_ref(x, wqkv, wo):
        B, S, D = x.shape
        H = 16
        Dh = D // H
        qkv = (lambda x, *d: x.permute(*d))((x @ wqkv).reshape(B, S, 3, H, Dh), 2, 0, 3, 1, 4)
        q, k, v = qkv[0], qkv[1], qkv[2]
        scores = torch.einsum("bhqd,bhkd->bhqk", q, k) * (Dh ** -0.5)
        p = _softmax(scores, axis=-1)
        o = torch.einsum("bhqk,bhkd->bhqd", p, v)
        return (lambda x, *d: x.permute(*d))(o, 0, 2, 1, 3).reshape(B, S, D) @ wo

    def _B_ref(u, A_re, A_im, B_re, B_im, C_re, C_im, log_dt, Dskip):
        Bsz, L, D = u.shape
        dt = torch.exp(log_dt)[:, None]
        A = A_re + 1j * A_im
        Bc = B_re + 1j * B_im
        Cc = C_re + 1j * C_im
        logdA = dt * A
        dB = dt * Bc
        l = (lambda x, dt: x.to(dt))(torch.arange(L), A_re.dtype)
        P = torch.exp(logdA[:, :, None] * l[None, None, :])
        K = torch.einsum("dn,dnl,dn->dl", Cc, P, dB).real
        L2 = 2 * L
        Kp = _pad(K, ((0, 0), (0, L)))
        up = (lambda x, *d: x.permute(*d))(u, 0, 2, 1)
        up = _pad(up, ((0, 0), (0, 0), (0, L)))
        Kf = (lambda x, axes=(-2,-1): torch.fft.rfft2(x.float(), dim=axes))(Kp[:, None, :], axes=(-2, -1))
        Uf = (lambda x, axes=(-2,-1): torch.fft.rfft2(x.float(), dim=axes))(up[:, :, None, :], axes=(-2, -1))
        Yf = Uf * Kf[None, :, :, :]
        y = (lambda x, s, axes=(-2,-1): torch.fft.irfft2(x, s=s, dim=axes).to(torch.bfloat16))(Yf, (1, L2), axes=(-2, -1))[:, :, 0, :L]
        y = (lambda x, *d: x.permute(*d))(y, 0, 2, 1) + u * Dskip[None, None, :]
        return y

    x = A_x
    g = _sigmoid((x * gate_w).sum(-1, keepdims=True))
    a = _A_ref(x, A_wqkv, A_wo)
    b = _B_ref(x, B_A_re, B_A_im, B_B_re, B_B_im, B_C_re, B_C_im, B_log_dt, B_Dskip)
    return g * a + (1 - g) * b
```

```cpp
#include <hip/hip_runtime.h>
#include <hip/hip_bf16.h>
#include <stdint.h>
typedef unsigned short u16;
__device__ __forceinline__ float bf2f(u16 v) { return __uint_as_float(((unsigned)v) << 16); }
__device__ __forceinline__ u16 f2bf(float f) { unsigned u = __float_as_uint(f); u += 0x7fffu + ((u >> 16) & 1u); return (u16)(u >> 16); }
__device__ __forceinline__ float rbf(float f) { return bf2f(f2bf(f)); }
constexpr int S = 2048, D = 448, H = 16, DH = 28, N = 256;

__global__ void k_qkv(const u16* x, const u16* w, u16* qkv) {
    int col = blockIdx.x * 64 + threadIdx.x; int row = blockIdx.y;
    if (col >= 3 * D) return;
    float acc = 0.f;
    for (int k = 0; k < D; ++k) acc += bf2f(x[row * D + k]) * bf2f(w[k * 3 * D + col]);
    qkv[row * 3 * D + col] = f2bf(acc);
}
__global__ void k_attn(const u16* qkv, u16* o) {
    int h = blockIdx.y; int q = blockIdx.x * 64 + threadIdx.x;
    float qv[DH]; for (int d = 0; d < DH; ++d) qv[d] = bf2f(qkv[q * 3 * D + h * DH + d]);
    float m = -1e30f, l = 0.f, acc[DH]; for (int d = 0; d < DH; ++d) acc[d] = 0.f;
    const float sc = rsqrtf((float)DH);
    for (int k = 0; k < S; ++k) {
        float s = 0.f; for (int d = 0; d < DH; ++d) s += qv[d] * bf2f(qkv[k * 3 * D + D + h * DH + d]);
        s *= sc; float mn = fmaxf(m, s); float al = __expf(m - mn), p = __expf(s - mn); l = l * al + p;
        for (int d = 0; d < DH; ++d) acc[d] = acc[d] * al + p * bf2f(qkv[k * 3 * D + 2 * D + h * DH + d]);
        m = mn;
    }
    for (int d = 0; d < DH; ++d) o[q * D + h * DH + d] = f2bf(acc[d] / l);
}
__global__ void k_kgen(const u16* Are, const u16* Aim, const u16* Bre, const u16* Bim, const u16* Cre, const u16* Cim, const u16* logdt, float* K) {
    int d = blockIdx.y; int l = blockIdx.x * 256 + threadIdx.x;
    float dt = rbf(expf(bf2f(logdt[d]))); float lf = rbf((float)l);
    float acc = 0.f;
    for (int n = 0; n < N; ++n) {
        float ar = dt * bf2f(Are[d * N + n]), ai = dt * bf2f(Aim[d * N + n]);
        float br = dt * bf2f(Bre[d * N + n]), bi = dt * bf2f(Bim[d * N + n]);
        float cr = bf2f(Cre[d * N + n]), ci = bf2f(Cim[d * N + n]);
        float e = expf(ar * lf), s, c; sincosf(ai * lf, &s, &c);
        float pr = e * c, pi = e * s;
        float wr = cr * br - ci * bi, wi = cr * bi + ci * br;
        acc += wr * pr - wi * pi;
    }
    K[d * S + l] = acc;
}
__global__ void k_conv(const u16* x, const float* K, float* y) {
    int d = blockIdx.y; int l = blockIdx.x * 256 + threadIdx.x;
    float acc = 0.f;
    for (int j = 0; j <= l; ++j) acc += K[d * S + j] * bf2f(x[(l - j) * D + d]);
    y[l * D + d] = acc;
}
__global__ void k_out(const u16* x, const u16* o, const u16* wo, const float* y, const u16* dskip, const u16* gw, u16* out) {
    int row = blockIdx.x; int col = threadIdx.x;
    __shared__ float red[512];
    float xv = col < D ? bf2f(x[row * D + col]) : 0.f;
    red[col] = col < D ? rbf(xv * bf2f(gw[col])) : 0.f;
    __syncthreads();
    for (int s = 256; s > 0; s >>= 1) { if (col < s) red[col] += red[col + s]; __syncthreads(); }
    float z = rbf(red[0]); float g = rbf(1.f / (1.f + expf(-z)));
    if (col >= D) return;
    float acc = 0.f;
    for (int k = 0; k < D; ++k) acc += bf2f(o[row * D + k]) * bf2f(wo[k * D + col]);
    float a = rbf(acc);
    float b = rbf(rbf(y[row * D + col]) + rbf(xv * bf2f(dskip[col])));
    out[row * D + col] = f2bf(rbf(g * a) + rbf(rbf(1.f - g) * b));
}
extern "C" void kernel_launch(void* const* d_in, const int* in_sizes, int n_in, void* d_out, int out_size, void* d_ws, size_t ws_size, hipStream_t stream) {
    const u16* x = (const u16*)d_in[0]; const u16* wqkv = (const u16*)d_in[1]; const u16* wo = (const u16*)d_in[2];
    char* ws = (char*)d_ws;
    u16* qkv = (u16*)ws; u16* o = (u16*)(ws + (8 << 20)); float* K = (float*)(ws + (16 << 20)); float* y = (float*)(ws + (24 << 20));
    k_qkv<<<dim3(21, S), 64, 0, stream>>>(x, wqkv, qkv);
    k_attn<<<dim3(S / 64, H), 64, 0, stream>>>(qkv, o);
    k_kgen<<<dim3(S / 256, D), 256, 0, stream>>>((const u16*)d_in[4], (const u16*)d_in[5], (const u16*)d_in[6], (const u16*)d_in[7], (const u16*)d_in[8], (const u16*)d_in[9], (const u16*)d_in[10], K);
    k_conv<<<dim3(S / 256, D), 256, 0, stream>>>(x, K, y);
    k_out<<<S, 512, 0, stream>>>(x, o, wo, y, (const u16*)d_in[11], (const u16*)d_in[12], (u16*)d_out);
}
```

```cpp
#include <hip/hip_runtime.h>
#include <hip/hip_bf16.h>
#include <stdint.h>

typedef unsigned short u16;
typedef __attribute__((ext_vector_type(8))) __bf16 bf16x8;
typedef __attribute__((ext_vector_type(2))) __bf16 bf16x2;
typedef __attribute__((ext_vector_type(4))) short s16x4;
typedef __attribute__((ext_vector_type(2))) float f32x2;
typedef __attribute__((ext_vector_type(4))) float f32x4;
typedef __attribute__((ext_vector_type(16))) float f32x16;
typedef __attribute__((ext_vector_type(2))) unsigned u32x2;
typedef __attribute__((ext_vector_type(4))) unsigned u32x4;
#define LAS __attribute__((address_space(3)))
#define DI __device__ __forceinline__

constexpr int S = 2048, D = 448, H = 16, NM = 256;
constexpr size_t WS_Q = 0;
constexpr size_t WS_K = 2u << 20;
constexpr size_t WS_V = 4u << 20;
constexpr size_t WS_U = 6u << 20;
constexpr size_t WS_KX = 8u << 20;
constexpr size_t WS_O = 10u << 20;
constexpr size_t WS_Y = 12u << 20;

#define ST_DECL
#define ST(i)
#define ST_FLUSH(base, n)

DI f32x16 mfma32(u32x4 a, u32x4 b, f32x16 c) { return __builtin_amdgcn_mfma_f32_32x32x16_bf16(__builtin_bit_cast(bf16x8, a), __builtin_bit_cast(bf16x8, b), c, 0, 0, 0); }
DI f32x4 mfma16(u32x4 a, u32x4 b, f32x4 c) { return __builtin_amdgcn_mfma_f32_16x16x32_bf16(__builtin_bit_cast(bf16x8, a), __builtin_bit_cast(bf16x8, b), c, 0, 0, 0); }
DI unsigned pk(float lo, float hi) { f32x2 v = {lo, hi}; return __builtin_bit_cast(unsigned, __builtin_convertvector(v, bf16x2)); }
DI float bf2f(unsigned v) { return __uint_as_float(v << 16); }
DI u32x2 trrd(LAS const char* p) { return __builtin_bit_cast(u32x2, __builtin_amdgcn_ds_read_tr16_b64_v4i16((LAS s16x4*)p)); }
DI u32x4 trrd2(LAS const char* p0, LAS const char* p1) { u32x2 a = trrd(p0), b = trrd(p1); return u32x4{a[0], a[1], b[0], b[1]}; }
DI void dma16(unsigned voff, const void* sbase, unsigned ldsdst) {
    unsigned keep;
    asm volatile("s_mov_b32 %0, m0\n\ts_mov_b32 m0, %3\n\ts_nop 0\n\tglobal_load_lds_dwordx4 %1, %2\n\ts_mov_b32 m0, %0" : "=&s"(keep) : "v"(voff), "s"(sbase), "s"(ldsdst) : "memory");
}
DI f32x2 cmul(f32x2 a, f32x2 b) { f32x2 bs = {-b[1], b[0]}; f32x2 ax = {a[0], a[0]}, ay = {a[1], a[1]}; return __builtin_elementwise_fma(ay, bs, ax * b); }
#define BARRIER() asm volatile("s_waitcnt lgkmcnt(0)\n\ts_barrier" ::: "memory")

constexpr int L1_W = 0, L1_U = 86016, L1_KG = 94208, L1_BYTES = 159744;

template <int V0, int V1, int V2>
DI void gemm_loop(f32x16 (&acc)[3], const u32x4 (&xf)[14], LAS const char* wp) {
#pragma unroll
    for (int ks = 0; ks < 14; ++ks) {
        const u32x4 w0 = trrd2(wp + ks * 3072, wp + ks * 3072 + 768);
        const u32x4 w1 = trrd2(wp + ks * 3072 + 64, wp + ks * 3072 + 768 + 64);
        const u32x4 w2 = trrd2(wp + ks * 3072 + 128, wp + ks * 3072 + 768 + 128);
        acc[0] = V0 ? mfma32(xf[ks], w0, acc[0]) : mfma32(w0, xf[ks], acc[0]);
        acc[1] = V1 ? mfma32(xf[ks], w1, acc[1]) : mfma32(w1, xf[ks], acc[1]);
        acc[2] = V2 ? mfma32(xf[ks], w2, acc[2]) : mfma32(w2, xf[ks], acc[2]);
    }
}

__global__ __launch_bounds__(512, 2) void k1(const u16* x, const u16* wqkv, const u16* Are, const u16* Aim, const u16* Bre, const u16* Bim,
                                             const u16* Cre, const u16* Cim, const u16* logdt, const u16* dskip, char* ws) {
    __shared__ __attribute__((aligned(16))) char lds_[L1_BYTES];
    LAS char* lds = (LAS char*)lds_;
    ST_DECL; ST(0);
    const unsigned lds0 = (unsigned)(uintptr_t)lds;
    const int tid = threadIdx.x, lane = tid & 63, w = __builtin_amdgcn_readfirstlane(tid >> 6);
    const int b = blockIdx.x, xcd = b & 7, bi = b >> 3;
    const int rt = 4 * (xcd >> 1) + (bi & 3), ct = 7 * (xcd & 1) + (bi >> 2);
    const int ch = 2 * b + (w >> 2), wq = w & 3;
    const int rb = w & 3, kh = w >> 2;
    const int r32 = lane & 31, hh = lane >> 5;
    unsigned pAr, pAi, pBr, pBi, pCr, pCi, pLd, pDs;
    {
        const unsigned po = (ch * NM + wq * 64 + lane) * 2, co = ch * 2;
        asm volatile("global_load_ushort %0, %8, %10\n\tglobal_load_ushort %1, %8, %11\n\tglobal_load_ushort %2, %8, %12\n\tglobal_load_ushort %3, %8, %13\n\t"
                     "global_load_ushort %4, %8, %14\n\tglobal_load_ushort %5, %8, %15\n\tglobal_load_ushort %6, %9, %16\n\tglobal_load_ushort %7, %9, %17"
                     : "=&v"(pAr), "=&v"(pAi), "=&v"(pBr), "=&v"(pBi), "=&v"(pCr), "=&v"(pCi), "=&v"(pLd), "=&v"(pDs)
                     : "v"(po), "v"(co), "s"(Are), "s"(Aim), "s"(Bre), "s"(Bim), "s"(Cre), "s"(Cim), "s"(logdt), "s"(dskip) : "memory");
    }
    u32x4 xf[14];
    {
        const unsigned xo = ((rt * 128 + rb * 32 + r32) * D + kh * 224 + hh * 8) * 2;
#define XL(i) asm volatile("global_load_dwordx4 %0, %1, %2 offset:" #i "*32" : "=v"(xf[i]) : "v"(xo), "s"(x) : "memory")
        XL(0); XL(1); XL(2); XL(3); XL(4); XL(5); XL(6); XL(7); XL(8); XL(9); XL(10); XL(11); XL(12); XL(13);
#undef XL
    }
    {
        unsigned vo[3];
#pragma unroll
        for (int r = 0; r < 3; ++r) { const unsigned o = 1024 * r + 16 * lane; vo[r] = (o / 192) * 2688 + (o % 192); }
        const char* wb = (const char*)wqkv + (size_t)(56 * w) * 2688 + ct * 192;
#pragma unroll
        for (int i = 0; i < 10; ++i) dma16(vo[i % 3], wb + (i / 3) * 16 * 2688, lds0 + L1_W + w * 10752 + i * 1024);
        if (lane < 32) dma16(vo[1], wb + 3 * 16 * 2688, lds0 + L1_W + w * 10752 + 10 * 1024);
        const unsigned uo = ((rt * 128 + 16 * w + (lane >> 2)) * D + ct * 32) * 2 + (lane & 3) * 16;
        dma16(uo, x, lds0 + L1_U + w * 1024);
    }
    asm volatile("s_waitcnt vmcnt(26)" : "+v"(pAr), "+v"(pAi), "+v"(pBr), "+v"(pBi), "+v"(pCr), "+v"(pCi), "+v"(pLd), "+v"(pDs) :: "memory");
    ST(1);
    {
        const float dt = bf2f(pk(expf(bf2f(pLd)), 0.f) & 0xffffu);
        const float ar = dt * bf2f(pAr), ai = dt * bf2f(pAi);
        const float lr = ar * 1.4426950408889634f;
        const float ph = ai * 0.15915494f, pe = __builtin_fmaf(ai, 0.15915494f, -ph) + ai * 6.4206383e-9f;
        auto zpow = [&](float e) { const float m = __builtin_amdgcn_exp2f(lr * e); const float t = ph * e; const float f = (t - __builtin_rintf(t)) + pe * e;
                                   return f32x2{m * __builtin_amdgcn_cosf(f), m * __builtin_amdgcn_sinf(f)}; };
        f32x2 z1 = zpow(1.f), Z1 = zpow(256.f); const f32x2 z8 = zpow(8.f);
        z1[1] = -z1[1]; Z1[1] = -Z1[1];
        LAS char* img = lds + L1_KG + w * 8192;
        const int sw = (lane >> 1) & 3;
        auto put8 = [&](int c, const f32x2 (&v)[8]) {
            const u32x4 re = {pk(v[0][0], v[1][0]), pk(v[2][0], v[3][0]), pk(v[4][0], v[5][0]), pk(v[6][0], v[7][0])};
            const u32x4 im = {pk(v[0][1], v[1][1]), pk(v[2][1], v[3][1]), pk(v[4][1], v[5][1]), pk(v[6][1], v[7][1])};
            *(LAS u32x4*)(img + lane * 64 + ((c ^ sw) << 4)) = re;
            *(LAS u32x4*)(img + 4096 + lane * 64 + ((c ^ sw) << 4)) = im; };
        {
            f32x2 v[8];
            v[0] = f32x2{1.f, 0.f}; v[1] = z1; v[2] = cmul(z1, z1); v[3] = cmul(v[2], z1); v[4] = cmul(v[2], v[2]); v[5] = cmul(v[4], z1); v[6] = cmul(v[4], v[2]); v[7] = cmul(v[4], v[3]);
            put8(0, v);
            f32x2 u[8];
            u[0] = Z1; u[1] = cmul(Z1, v[2]); u[2] = cmul(Z1, v[4]); u[3] = cmul(Z1, v[6]); u[4] = cmul(Z1, Z1); u[5] = cmul(u[4], v[4]); u[6] = cmul(u[4], Z1); u[7] = cmul(u[6], v[4]);
            put8(1, u);
            f32x2 t[8];
            t[0] = cmul(u[4], u[4]); t[1] = cmul(t[0], Z1); t[2] = cmul(t[0], u[4]); t[3] = cmul(t[0], u[6]); t[4] = cmul(t[0], t[0]); t[5] = f32x2{0.f, 0.f}; t[6] = t[5]; t[7] = t[5];
            put8(2, t);
        }
        const int G = lane >> 4, q = (lane & 15) >> 2, p = lane & 3, pp = 4 * (G & 1) + p;
        LAS const char* r0 = img + (8 * hh + q) * 64 + (((pp >> 1) ^ (q >> 1)) << 4) + (pp & 1) * 8;
        LAS const char* r1 = img + (8 * hh + 4 + q) * 64 + (((pp >> 1) ^ (2 + (q >> 1))) << 4) + (pp & 1) * 8;
        u32x4 bf[8];
#pragma unroll
        for (int ks = 0; ks < 8; ++ks) bf[ks] = trrd2(r0 + ks * 1024, r1 + ks * 1024);
        const f32x2 cb = cmul(f32x2{bf2f(pCr), bf2f(pCi)}, f32x2{dt * bf2f(pBr), dt * bf2f(pBi)});
        const f32x2 z16 = cmul(z8, z8);
        f32x2 e0 = cb, e1 = cmul(cb, z8);
#pragma unroll
        for (int c = 0; c < 4; ++c) {
            f32x2 v[8];
#pragma unroll
            for (int j = 0; j < 4; ++j) { v[2 * j] = e0; v[2 * j + 1] = e1; if (c < 3 || j < 3) { e0 = cmul(e0, z16); e1 = cmul(e1, z16); } }
            put8(c, v);
        }
        f32x16 kc = {};
#pragma unroll
        for (int ks = 0; ks < 8; ++ks) kc = mfma32(bf[ks], trrd2(r0 + ks * 1024, r1 + ks * 1024), kc);
#pragma unroll
        for (int g = 0; g < 3; ++g) *(LAS f32x4*)(img + r32 * 96 + g * 32 + hh * 16) = f32x4{kc[4 * g], kc[4 * g + 1], kc[4 * g + 2], kc[4 * g + 3]};
    }
    ST(2);
    asm volatile("s_waitcnt vmcnt(0) lgkmcnt(0)\n\ts_barrier"
                 : "+v"(xf[0]), "+v"(xf[1]), "+v"(xf[2]), "+v"(xf[3]), "+v"(xf[4]), "+v"(xf[5]), "+v"(xf[6]), "+v"(xf[7]), "+v"(xf[8]), "+v"(xf[9]), "+v"(xf[10]), "+v"(xf[11]), "+v"(xf[12]), "+v"(xf[13]) :: "memory");
    ST(3);
    f32x16 acc[3] = {};
    {
        const int G = lane >> 4, q = (lane & 15) >> 2, p = lane & 3, p2 = ((p & 1) << 1) | (p >> 1);
        LAS const char* wp = lds + L1_W + (kh * 224 + 8 * hh + q) * 192 + (G & 1) * 32 + p2 * 8;
        if (ct <= 8) gemm_loop<0, 0, 0>(acc, xf, wp); else if (ct == 9) gemm_loop<0, 1, 1>(acc, xf, wp); else gemm_loop<1, 1, 1>(acc, xf, wp);
    }
    ST(4);
    BARRIER();
    {
        LAS char* mine = lds + L1_W + w * 6144 + lane * 16;
        LAS const char* other = lds + L1_W + (w ^ 4) * 6144 + lane * 16;
        float keep[3][8];
#pragma unroll
        for (int k = 0; k < 3; ++k) {
            f32x4 s0, s1;
#pragma unroll
            for (int j = 0; j < 4; ++j) { s0[j] = kh ? acc[k][j] : acc[k][8 + j]; s1[j] = kh ? acc[k][4 + j] : acc[k][12 + j]; }
#pragma unroll
            for (int j = 0; j < 8; ++j) keep[k][j] = kh ? acc[k][8 + j] : acc[k][j];
            *(LAS f32x4*)(mine + k * 2048) = s0; *(LAS f32x4*)(mine + k * 2048 + 1024) = s1;
        }
        BARRIER();
        const int tile = rt * 4 + rb;
#pragma unroll
        for (int k = 0; k < 3; ++k) {
            const f32x4 o0 = *(LAS const f32x4*)(other + k * 2048), o1 = *(LAS const f32x4*)(other + k * 2048 + 1024);
            const int blk = 3 * ct + k;
            const float sc = blk < 14 ? 0.27264374f : 1.f;
            const u32x4 v = {pk((keep[k][0] + o0[0]) * sc, (keep[k][1] + o0[1]) * sc), pk((keep[k][2] + o0[2]) * sc, (keep[k][3] + o0[3]) * sc),
                             pk((keep[k][4] + o1[0]) * sc, (keep[k][5] + o1[1]) * sc), pk((keep[k][6] + o1[2]) * sc, (keep[k][7] + o1[3]) * sc)};
            if (blk < 28) {
                const int part = blk >= 14, gb = (blk - 14 * part) * 4 + 2 * kh;
                *(u32x4*)(ws + (part ? WS_K : WS_Q) + (size_t)(tile * 56 + gb) * 512 + lane * 16) = v;
            } else {
                const int col = (blk - 28) * 32 + ((r32 & 19) | ((r32 & 4) << 1) | ((r32 & 8) >> 1));
                const int hd = col / 28, dd = col - hd * 28, slot = (dd & 19) | ((dd & 4) << 1) | ((dd & 8) >> 1);
                char* dst = ws + WS_V + (size_t)((hd * 64 + tile) * 2 + kh) * 1024 + hh * 512;
                *(u32x4*)(dst + slot * 16) = v;
                if (dd == 27) *(u32x4*)(dst + 28 * 16) = u32x4{0x3f803f80u, 0x3f803f80u, 0x3f803f80u, 0x3f803f80u};
            }
        }
    }
    ST(5);
    {
        const int G = lane >> 4, q = (lane & 15) >> 2, p = lane & 3, rho = w & 3, n0 = 16 * (w >> 2) + 8 * hh;
        LAS const char* up = lds + L1_U + (4 * (n0 + q) + rho) * 64 + (G & 1) * 32 + p * 8;
        const u32x4 v = trrd2(up, up + 16 * 64);
        *(u32x4*)(ws + WS_U + (size_t)(ct * 32 + r32) * 4096 + rt * 256 + rho * 64 + n0 * 2) = v;
    }
    {
        LAS const char* T = lds + L1_KG + (w & 4) * 8192;
        const int i = r32, sec = 2 * wq + hh;
        float o[8];
        if (wq == 0) {
            const int a0 = i * 96 + hh * 32, a1 = hh ? (i < 31 ? (i + 1) * 96 + 32 : 48) : i * 96 + 16;
            f32x4 v = {}, n = {};
#pragma unroll
            for (int k = 0; k < 4; ++k) { v += *(LAS const f32x4*)(T + k * 8192 + a0); n += *(LAS const f32x4*)(T + k * 8192 + a1); }
            o[0] = v[0]; o[1] = hh ? v[0] : v[1]; o[2] = hh ? v[1] : v[2]; o[3] = hh ? v[2] : v[3];
            o[4] = hh ? v[2] : n[0]; o[5] = hh ? v[2] : n[1]; o[6] = hh ? v[3] : n[2]; o[7] = hh ? n[0] : n[3];
            if (lane == 0) o[0] += bf2f(pDs);
        } else if (wq == 1) {
            const int row = 12 + 2 * hh, a0 = i * 96 + row * 4, a1 = i < 31 ? a0 + 96 : (row + 2) * 4;
            f32x2 v = {}; float n = 0.f;
#pragma unroll
            for (int k = 0; k < 4; ++k) { v += *(LAS const f32x2*)(T + k * 8192 + a0); n += *(LAS const float*)(T + k * 8192 + a1); }
            o[0] = o[1] = o[2] = v[0]; o[3] = o[4] = o[5] = v[1]; o[6] = o[7] = n;
        } else {
            const int row = 12 + sec, a0 = i * 96 + row * 4, a1 = i < 31 ? a0 + 96 : (row + 1) * 4;
            float v = 0.f, n = 0.f;
#pragma unroll
            for (int k = 0; k < 4; ++k) { v += *(LAS const float*)(T + k * 8192 + a0); n += *(LAS const float*)(T + k * 8192 + a1); }
            o[0] = o[1] = o[2] = o[3] = v; o[4] = (i & 1) ? n : v; o[5] = o[6] = o[7] = n;
        }
        *(u32x4*)(ws + WS_KX + (size_t)ch * 4096 + (wq * 64 + lane) * 16) = u32x4{pk(o[0], o[1]), pk(o[2], o[3]), pk(o[4], o[5]), pk(o[6], o[7])};
    }
    ST(6);
    ST_FLUSH(0, 7);
}

#define QT 2
constexpr int NKG = 2 * QT;
constexpr int KT = 64 / NKG;
constexpr int L2_KX = 0;
constexpr int L2_U = 8448;
constexpr int L2_R = 20736;
constexpr int L2_L = L2_R + (QT == 2 ? 65536 + 4096 : 8 * QT * 4096);
constexpr int L2_F = L2_L + 8 * QT * 128;
constexpr int L2_BYTES = L2_F + 16;

template <int MODE>
DI void attn_loop(f32x16 (&o)[QT], const u32x4 (&qf)[QT][2], float (&mx)[QT], const char* kp, const char* vp, unsigned lo) {
    u32x4 k0 = *(const u32x4*)(kp + lo), k1 = *(const u32x4*)(kp + lo + 1024);
    u32x4 v0 = *(const u32x4*)(vp + lo), v1 = *(const u32x4*)(vp + lo + 1024);
#pragma unroll 2
    for (int kt = 0; kt < KT; ++kt) {
        const int kn = kt + 1 < KT ? kt + 1 : kt;
        const u32x4 nk0 = *(const u32x4*)(kp + (size_t)kn * 28672 + lo), nk1 = *(const u32x4*)(kp + (size_t)kn * 28672 + lo + 1024);
        const u32x4 nv0 = *(const u32x4*)(vp + (size_t)kn * 2048 + lo), nv1 = *(const u32x4*)(vp + (size_t)kn * 2048 + lo + 1024);
#pragma unroll
        for (int t = 0; t < QT; ++t) {
            f32x16 s = mfma32(k0, qf[t][0], f32x16{});
            s = mfma32(k1, qf[t][1], s);
            if (MODE == 1) {
#pragma unroll
                for (int r = 0; r < 16; ++r) mx[t] = __builtin_fmaxf(mx[t], s[r]);
            } else {
#pragma unroll
                for (int r = 0; r < 16; ++r) s[r] = __builtin_amdgcn_exp2f(MODE == 2 ? s[r] - mx[t] : s[r]);
                const u32x4 p0 = {pk(s[0], s[1]), pk(s[2], s[3]), pk(s[4], s[5]), pk(s[6], s[7])};
                const u32x4 p1 = {pk(s[8], s[9]), pk(s[10], s[11]), pk(s[12], s[13]), pk(s[14], s[15])};
                o[t] = mfma32(v0, p0, o[t]);
                o[t] = mfma32(v1, p1, o[t]);
            }
        }
        k0 = nk0; k1 = nk1; v0 = nv0; v1 = nv1;
    }
}

__global__ __launch_bounds__(512, 2) void k2(char* ws) {
    __shared__ __attribute__((aligned(16))) char lds_[L2_BYTES];
    LAS char* lds = (LAS char*)lds_;
    ST_DECL; ST(0);
    const int tid = threadIdx.x, lane = tid & 63, w = __builtin_amdgcn_readfirstlane(tid >> 6);
    const int b = blockIdx.x, hd = 2 * (b & 7) + ((b >> 3) & 1), rt = b >> 4;
    const int r32 = lane & 31, hh = lane >> 5;
    const int qg = w / NKG, kg = w % NKG;
    const int g0 = (7 * hd) >> 1;
    const bool conv = b < 224;
    if (conv) {
        const int c = w >> 2, t = tid & 255, chn = 2 * b + c;
        const u32x4 kv = *(const u32x4*)(ws + WS_KX + (size_t)chn * 4096 + t * 16);
        const u32x4 uv = *(const u32x4*)(ws + WS_U + (size_t)chn * 4096 + t * 16);
        *(LAS u32x4*)(lds + L2_KX + c * 4224 + 64 + t * 16) = kv;
        if (t < 4) *(LAS u32x4*)(lds + L2_KX + c * 4224 + t * 16) = u32x4{0, 0, 0, 0};
        const int rho = (t >> 2) & 3, chunk = (t >> 4) * 4 + (t & 3);
        *(LAS u32x4*)(lds + L2_U + (c * 4 + rho) * 1536 + 512 + chunk * 16) = uv;
        if (t < 128) *(LAS u32x4*)(lds + L2_U + (c * 4 + (t >> 5)) * 1536 + (t & 31) * 16) = u32x4{0, 0, 0, 0};
    }
    if (tid == 0) *(LAS int*)(lds + L2_F) = 0;
    u32x4 qf[QT][2];
#pragma unroll
    for (int t = 0; t < QT; ++t) {
        const char* qp = ws + WS_Q + (size_t)((rt * 4 + qg * QT + t) * 56 + g0) * 512 + lane * 16;
        qf[t][0] = *(const u32x4*)qp; qf[t][1] = *(const u32x4*)(qp + 1024);
        if (hd & 1) { if (!hh) { qf[t][0][0] = 0; qf[t][0][1] = 0; } } else { if (hh) { qf[t][1][2] = 0; qf[t][1][3] = 0; } }
    }
    const char* kp = ws + WS_K + (size_t)(kg * KT * 56 + g0) * 512;
    const char* vp = ws + WS_V + (size_t)(hd * 64 + kg * KT) * 2048;
    __syncthreads();
    ST(1);
    if (conv) {
        const int c = w >> 2, rho = w & 3;
        const int G = lane >> 4, q = (lane & 15) >> 2, p = lane & 3;
        LAS const char* ap = lds + L2_KX + c * 4224 + (16 - 8 * hh - q + 4 * (G & 1) + p) * 8;
        LAS const char* bp = lds + L2_U + (c * 4 + rho) * 1536 + (32 + r32 - 1 + hh) * 16;
        f32x16 y0 = {}, y1 = {};
#pragma unroll 4
        for (int g = 0; g < 16; ++g) {
            const u32x4 a0 = trrd2(ap + g * 128, ap + g * 128 - 32), a1 = trrd2(ap + g * 128 + 2048, ap + g * 128 + 2048 - 32);
            const u32x4 b0 = *(LAS const u32x4*)(bp - g * 32), b1 = *(LAS const u32x4*)(bp - g * 32 + 512);
            y0 = mfma32(a0, b0, y0); y1 = mfma32(a0, b1, y1); y1 = mfma32(a1, b0, y1);
        }
        LAS char* pl = lds + L2_R + (c * 4 + rho) * 8704 + 16;
#pragma unroll
        for (int g = 0; g < 4; ++g) {
            *(LAS f32x4*)(pl + (r32 * 32 + 8 * g + 4 * hh) * 4) = f32x4{y0[4 * g], y0[4 * g + 1], y0[4 * g + 2], y0[4 * g + 3]};
            *(LAS f32x4*)(pl + 4096 + (r32 * 32 + 8 * g + 4 * hh) * 4) = f32x4{y1[4 * g], y1[4 * g + 1], y1[4 * g + 2], y1[4 * g + 3]};
        }
        if (lane == 0) *(LAS f32x4*)(pl - 16) = f32x4{0.f, 0.f, 0.f, 0.f};
        __syncthreads();
        const int t = tid & 255;
        LAS const char* rp = lds + L2_R + (c * 4) * 8704 + t * 32;
        float y[8];
        { const f32x4 m = *(LAS const f32x4*)(rp + 16), n = *(LAS const f32x4*)(rp + 32);
#pragma unroll
          for (int j = 0; j < 4; ++j) { y[j] = m[j]; y[4 + j] = n[j]; } }
#pragma unroll
        for (int r = 1; r < 4; ++r) {
            const f32x4 l = *(LAS const f32x4*)(rp + r * 8704), m = *(LAS const f32x4*)(rp + r * 8704 + 16), n = *(LAS const f32x4*)(rp + r * 8704 + 32);
            float e[12];
#pragma unroll
            for (int j = 0; j < 4; ++j) { e[j] = l[j]; e[4 + j] = m[j]; e[8 + j] = n[j]; }
#pragma unroll
            for (int j = 0; j < 8; ++j) y[j] += e[4 + j - r];
        }
        *(u32x4*)(ws + WS_Y + (size_t)(2 * b + c) * 4096 + t * 16) = u32x4{pk(y[0], y[1]), pk(y[2], y[3]), pk(y[4], y[5]), pk(y[6], y[7])};
        __syncthreads();
    }
    ST(2);
    f32x16 o[QT]; float mx[QT];
#pragma unroll
    for (int t = 0; t < QT; ++t) { o[t] = f32x16{}; mx[t] = 0.f; }
    attn_loop<0>(o, qf, mx, kp, vp, lane * 16);
    ST(3);
    bool done = false;
    for (int pass = 0; pass < 2; ++pass) {
        LAS char* wr = lds + L2_R + w * (QT * 4096) + lane * 16;
#pragma unroll
        for (int t = 0; t < QT; ++t) {
#pragma unroll
            for (int g = 0; g < 4; ++g) *(LAS f32x4*)(wr + (t * 4 + g) * 1024) = f32x4{o[t][4 * g], o[t][4 * g + 1], o[t][4 * g + 2], o[t][4 * g + 3]};
        }
        __syncthreads();
        const int ut = kg >> 1, us = kg & 1;
        LAS const char* rdp = lds + L2_R + qg * NKG * (QT * 4096) + ut * 4096;
        f32x4 a0 = {}, a1 = {}; float l = 0.f;
#pragma unroll
        for (int k = 0; k < NKG; ++k) {
            a0 += *(LAS const f32x4*)(rdp + k * (QT * 4096) + us * 2048 + lane * 16);
            a1 += *(LAS const f32x4*)(rdp + k * (QT * 4096) + us * 2048 + 1024 + lane * 16);
            l += *(LAS const float*)(rdp + k * (QT * 4096) + 3072 + (32 + r32) * 16);
        }
        if (us && hh) a1 = f32x4{0.f, 0.f, 0.f, 0.f};
        const float am = __builtin_fmaxf(__builtin_fmaxf(__builtin_fmaxf(__builtin_fabsf(a0[0]), __builtin_fabsf(a0[1])), __builtin_fmaxf(__builtin_fabsf(a0[2]), __builtin_fabsf(a0[3]))),
                                         __builtin_fmaxf(__builtin_fmaxf(__builtin_fabsf(a1[0]), __builtin_fabsf(a1[1])), __builtin_fmaxf(__builtin_fabsf(a1[2]), __builtin_fabsf(a1[3]))));
        const float chk = (a0[0] + a0[1] + a0[2] + a0[3] + a1[0] + a1[1] + a1[2] + a1[3]) * 0.f;
        const bool good = l >= 0x1p-20f && l <= 0x1p80f && am <= 0x1p120f && chk == 0.f;
        const bool allgood = __all(good) || pass == 1;
        if (allgood && !done) {
            const float rl = 1.f / l;
            const u32x4 v = {pk(a0[0] * rl, a0[1] * rl), pk(a0[2] * rl, a0[3] * rl), pk(a1[0] * rl, a1[1] * rl), pk(a1[2] * rl, a1[3] * rl)};
            *(u32x4*)(ws + WS_O + (size_t)(((rt * 4 + qg * QT + ut) * 16 + hd) * 4 + 2 * us) * 512 + lane * 16) = v;
            done = true;
        }
        if (pass == 1) break;
        if (!allgood && lane == 0) *(LAS int*)(lds + L2_F) = 1;
        __syncthreads();
        if (*(LAS volatile int*)(lds + L2_F) == 0) break;
#pragma unroll
        for (int t = 0; t < QT; ++t) { o[t] = f32x16{}; mx[t] = -__builtin_inff(); }
        attn_loop<1>(o, qf, mx, kp, vp, lane * 16);
#pragma unroll
        for (int t = 0; t < QT; ++t) { mx[t] = __builtin_fmaxf(mx[t], __shfl_xor(mx[t], 32)); if (!hh) *(LAS float*)(lds + L2_L + ((w * QT + t) * 32 + r32) * 4) = mx[t]; }
        __syncthreads();
#pragma unroll
        for (int t = 0; t < QT; ++t)
            for (int k = 0; k < NKG; ++k) mx[t] = __builtin_fmaxf(mx[t], *(LAS const float*)(lds + L2_L + (((qg * NKG + k) * QT + t) * 32 + r32) * 4));
        attn_loop<2>(o, qf, mx, kp, vp, lane * 16);
    }
    ST(4);
    ST_FLUSH(7, 5);
}

constexpr int L3_W = 0;
constexpr int L3_O = 101376;
constexpr int L3_Y = L3_O + 32768;
constexpr int L3_G = L3_Y + 7168;
constexpr int L3_BYTES = L3_G + 128;

__global__ __launch_bounds__(512, 2) void k3(const u16* x, const u16* wo, const u16* gw, char* ws, u16* out) {
    __shared__ __attribute__((aligned(16))) char lds_[L3_BYTES];
    LAS char* lds = (LAS char*)lds_;
    ST_DECL; ST(0);
    const unsigned lds0 = (unsigned)(uintptr_t)lds;
    const int tid = threadIdx.x, lane = tid & 63, w = __builtin_amdgcn_readfirstlane(tid >> 6);
    const int b = blockIdx.x, xcd = b & 7, bi = b >> 3;
    const int ct = bi & 3, rt = xcd * 8 + (bi >> 2);
    for (int i = w; i < 98; i += 8) {
        const unsigned o = (i % 7) * 1024 + lane * 16;
        dma16((o / 224) * 896 + (o % 224), (const char*)wo + (size_t)(i / 7) * 32 * 896 + ct * 224, lds0 + L3_W + i * 1024);
    }
#pragma unroll
    for (int i = 0; i < 4; ++i) dma16(lane * 16, ws + WS_O + (size_t)rt * 32768 + (w * 4 + i) * 1024, lds0 + L3_O + (w * 4 + i) * 1024);
    if (w < 7) dma16((16 * w + (lane >> 2)) * 4096 + (lane & 3) * 16, ws + WS_Y + (size_t)ct * 112 * 4096 + rt * 64, lds0 + L3_Y + w * 1024);
    if (tid < 56) *(LAS u32x4*)(lds + L3_W + 448 * 224 + tid * 16) = u32x4{0, 0, 0, 0};
    const int r16 = lane & 15, c4 = lane >> 4;
    if (w == 7) {
        f32x4 z0 = {}, z1 = {};
        const u16* xp = x + (size_t)(rt * 32 + r16) * D + c4 * 8;
#pragma unroll
        for (int ks = 0; ks < 14; ++ks) {
            const u32x4 gf = *(const u32x4*)(gw + ks * 32 + c4 * 8);
            z0 = mfma16(gf, *(const u32x4*)(xp + ks * 32), z0);
            z1 = mfma16(gf, *(const u32x4*)(xp + 16 * D + ks * 32), z1);
        }
        if (lane < 32) { const float z = lane < 16 ? z0[0] : z1[0]; *(LAS float*)(lds + L3_G + lane * 4) = 1.f / (1.f + __expf(-z)); }
    }
    asm volatile("s_waitcnt vmcnt(0) lgkmcnt(0)\n\ts_barrier" ::: "memory");
    ST(1);
    if (w < 7) {
        const int q = r16 >> 2, p = lane & 3;
        LAS const char* wp = lds + L3_W + (8 * c4 + q) * 224 + w * 32 + p * 8;
        LAS const char* op = lds + L3_O + c4 * 512 + r16 * 16;
        f32x4 c0 = {}, c1 = {};
#pragma unroll
        for (int hd = 0; hd < 16; ++hd) {
            const u32x4 a = trrd2(wp + hd * 28 * 224, wp + hd * 28 * 224 + 4 * 224);
            c0 = mfma16(a, *(LAS const u32x4*)(op + hd * 2048), c0);
            c1 = mfma16(a, *(LAS const u32x4*)(op + hd * 2048 + 256), c1);
        }
        LAS const char* yp = lds + L3_Y + (16 * w + 4 * c4 + q) * 64 + p * 8;
        const u32x2 y0 = trrd(yp), y1 = trrd(yp + 32);
        const float g0 = *(LAS const float*)(lds + L3_G + r16 * 4), g1 = *(LAS const float*)(lds + L3_G + 64 + r16 * 4);
        auto mix = [](float g, float a, float bb) { return g * a + (1.f - g) * bb; };
        u16* op0 = out + (size_t)(rt * 32 + r16) * D + ct * 112 + w * 16 + c4 * 4;
        *(u32x2*)op0 = u32x2{pk(mix(g0, c0[0], bf2f(y0[0] & 0xffffu)), mix(g0, c0[1], __uint_as_float(y0[0] & 0xffff0000u))),
                             pk(mix(g0, c0[2], bf2f(y0[1] & 0xffffu)), mix(g0, c0[3], __uint_as_float(y0[1] & 0xffff0000u)))};
        *(u32x2*)(op0 + 16 * D) = u32x2{pk(mix(g1, c1[0], bf2f(y1[0] & 0xffffu)), mix(g1, c1[1], __uint_as_float(y1[0] & 0xffff0000u))),
                                        pk(mix(g1, c1[2], bf2f(y1[1] & 0xffffu)), mix(g1, c1[3], __uint_as_float(y1[1] & 0xffff0000u)))};
    }
    ST(2);
    ST_FLUSH(12, 3);
}

extern "C" void kernel_launch(void* const* d_in, const int* in_sizes, int n_in, void* d_out, int out_size, void* d_ws, size_t ws_size, hipStream_t stream) {
    const u16* x = (const u16*)d_in[0];
    char* ws = (char*)d_ws;
    k1<<<224, 512, 0, stream>>>(x, (const u16*)d_in[1], (const u16*)d_in[4], (const u16*)d_in[5], (const u16*)d_in[6], (const u16*)d_in[7],
                                (const u16*)d_in[8], (const u16*)d_in[9], (const u16*)d_in[10], (const u16*)d_in[11], ws);
    k2<<<256, 512, 0, stream>>>(ws);
    k3<<<256, 512, 0, stream>>>(x, (const u16*)d_in[2], (const u16*)d_in[12], ws, (u16*)d_out);
}
```

```cpp
#include <hip/hip_runtime.h>
#include <hip/hip_bf16.h>
#include <stdint.h>

typedef unsigned short u16;
typedef __attribute__((ext_vector_type(8))) __bf16 bf16x8;
typedef __attribute__((ext_vector_type(2))) __bf16 bf16x2;
typedef __attribute__((ext_vector_type(4))) short s16x4;
typedef __attribute__((ext_vector_type(2))) float f32x2;
typedef __attribute__((ext_vector_type(4))) float f32x4;
typedef __attribute__((ext_vector_type(16))) float f32x16;
typedef __attribute__((ext_vector_type(2))) unsigned u32x2;
typedef __attribute__((ext_vector_type(4))) unsigned u32x4;
#define LAS __attribute__((address_space(3)))
#define DI __device__ __forceinline__

constexpr int S = 2048, D = 448, H = 16, NM = 256;
constexpr size_t WS_Q = 0;
constexpr size_t WS_K = 2u << 20;
constexpr size_t WS_V = 4u << 20;
constexpr size_t WS_U = 6u << 20;
constexpr size_t WS_KX = 8u << 20;
constexpr size_t WS_O = 10u << 20;
constexpr size_t WS_Y = 12u << 20;

#define ST_DECL
#define ST(i)
#define ST_FLUSH(base, n)

DI f32x16 mfma32(u32x4 a, u32x4 b, f32x16 c) { return __builtin_amdgcn_mfma_f32_32x32x16_bf16(__builtin_bit_cast(bf16x8, a), __builtin_bit_cast(bf16x8, b), c, 0, 0, 0); }
DI f32x4 mfma16(u32x4 a, u32x4 b, f32x4 c) { return __builtin_amdgcn_mfma_f32_16x16x32_bf16(__builtin_bit_cast(bf16x8, a), __builtin_bit_cast(bf16x8, b), c, 0, 0, 0); }
DI unsigned pk(float lo, float hi) { f32x2 v = {lo, hi}; return __builtin_bit_cast(unsigned, __builtin_convertvector(v, bf16x2)); }
DI float bf2f(unsigned v) { return __uint_as_float(v << 16); }
DI u32x2 trrd(LAS const char* p) { return __builtin_bit_cast(u32x2, __builtin_amdgcn_ds_read_tr16_b64_v4i16((LAS s16x4*)p)); }
DI u32x4 trrd2(LAS const char* p0, LAS const char* p1) { u32x2 a = trrd(p0), b = trrd(p1); return u32x4{a[0], a[1], b[0], b[1]}; }
DI void dma16(unsigned voff, const void* sbase, unsigned ldsdst) {
    unsigned keep;
    asm volatile("s_mov_b32 %0, m0\n\ts_mov_b32 m0, %3\n\ts_nop 0\n\tglobal_load_lds_dwordx4 %1, %2\n\ts_mov_b32 m0, %0" : "=&s"(keep) : "v"(voff), "s"(sbase), "s"(ldsdst) : "memory");
}
DI f32x2 cmul(f32x2 a, f32x2 b) { f32x2 bs = {-b[1], b[0]}; f32x2 ax = {a[0], a[0]}, ay = {a[1], a[1]}; return __builtin_elementwise_fma(ay, bs, ax * b); }
#define BARRIER() asm volatile("s_waitcnt lgkmcnt(0)\n\ts_barrier" ::: "memory")

constexpr int L1_W = 0, L1_U = 86016, L1_KG = 94208, L1_BYTES = 159744;

template <int V0, int V1, int V2>
DI void gemm_loop(f32x16 (&acc)[3], const u32x4 (&xf)[14], LAS const char* wp) {
#pragma unroll
    for (int ks = 0; ks < 14; ++ks) {
        const u32x4 w0 = trrd2(wp + ks * 3072, wp + ks * 3072 + 768);
        const u32x4 w1 = trrd2(wp + ks * 3072 + 64, wp + ks * 3072 + 768 + 64);
        const u32x4 w2 = trrd2(wp + ks * 3072 + 128, wp + ks * 3072 + 768 + 128);
        acc[0] = V0 ? mfma32(xf[ks], w0, acc[0]) : mfma32(w0, xf[ks], acc[0]);
        acc[1] = V1 ? mfma32(xf[ks], w1, acc[1]) : mfma32(w1, xf[ks], acc[1]);
        acc[2] = V2 ? mfma32(xf[ks], w2, acc[2]) : mfma32(w2, xf[ks], acc[2]);
    }
}

__global__ __launch_bounds__(512, 2) void k1(const u16* x, const u16* wqkv, const u16* Are, const u16* Aim, const u16* Bre, const u16* Bim,
                                             const u16* Cre, const u16* Cim, const u16* logdt, const u16* dskip, char* ws) {
    __shared__ __attribute__((aligned(16))) char lds_[L1_BYTES];
    LAS char* lds = (LAS char*)lds_;
    ST_DECL; ST(0);
    const unsigned lds0 = (unsigned)(uintptr_t)lds;
    const int tid = threadIdx.x, lane = tid & 63, w = __builtin_amdgcn_readfirstlane(tid >> 6);
    const int b = blockIdx.x, xcd = b & 7, bi = b >> 3;
    const int rt = 4 * (xcd >> 1) + (bi & 3), ct = 7 * (xcd & 1) + (bi >> 2);
    const int ch = 2 * b + (w >> 2), wq = w & 3;
    const int rb = w & 3, kh = w >> 2;
    const int r32 = lane & 31, hh = lane >> 5;
    unsigned pAr, pAi, pBr, pBi, pCr, pCi, pLd, pDs;
    {
        const unsigned po = (ch * NM + wq * 64 + lane) * 2, co = ch * 2;
        asm volatile("global_load_ushort %0, %8, %10\n\tglobal_load_ushort %1, %8, %11\n\tglobal_load_ushort %2, %8, %12\n\tglobal_load_ushort %3, %8, %13\n\t"
                     "global_load_ushort %4, %8, %14\n\tglobal_load_ushort %5, %8, %15\n\tglobal_load_ushort %6, %9, %16\n\tglobal_load_ushort %7, %9, %17"
                     : "=&v"(pAr), "=&v"(pAi), "=&v"(pBr), "=&v"(pBi), "=&v"(pCr), "=&v"(pCi), "=&v"(pLd), "=&v"(pDs)
                     : "v"(po), "v"(co), "s"(Are), "s"(Aim), "s"(Bre), "s"(Bim), "s"(Cre), "s"(Cim), "s"(logdt), "s"(dskip) : "memory");
    }
    u32x4 xf[14];
    {
        const unsigned xo = ((rt * 128 + rb * 32 + r32) * D + kh * 224 + hh * 8) * 2;
#define XL(i) asm volatile("global_load_dwordx4 %0, %1, %2 offset:" #i "*32" : "=v"(xf[i]) : "v"(xo), "s"(x) : "memory")
        XL(0); XL(1); XL(2); XL(3); XL(4); XL(5); XL(6); XL(7); XL(8); XL(9); XL(10); XL(11); XL(12); XL(13);
#undef XL
    }
    {
        unsigned vo[3];
#pragma unroll
        for (int r = 0; r < 3; ++r) { const unsigned o = 1024 * r + 16 * lane; vo[r] = (o / 192) * 2688 + (o % 192); }
        const char* wb = (const char*)wqkv + (size_t)(56 * w) * 2688 + ct * 192;
#pragma unroll
        for (int i = 0; i < 10; ++i) dma16(vo[i % 3], wb + (i / 3) * 16 * 2688, lds0 + L1_W + w * 10752 + i * 1024);
        if (lane < 32) dma16(vo[1], wb + 3 * 16 * 2688, lds0 + L1_W + w * 10752 + 10 * 1024);
        const unsigned uo = ((rt * 128 + 16 * w + (lane >> 2)) * D + ct * 32) * 2 + (lane & 3) * 16;
        dma16(uo, x, lds0 + L1_U + w * 1024);
    }
    asm volatile("s_waitcnt vmcnt(26)" : "+v"(pAr), "+v"(pAi), "+v"(pBr), "+v"(pBi), "+v"(pCr), "+v"(pCi), "+v"(pLd), "+v"(pDs) :: "memory");
    ST(1);
    {
        const float dt = bf2f(pk(expf(bf2f(pLd)), 0.f) & 0xffffu);
        const float ar = dt * bf2f(pAr), ai = dt * bf2f(pAi);
        const float lr = ar * 1.4426950408889634f;
        const float ph = ai * 0.15915494f, pe = __builtin_fmaf(ai, 0.15915494f, -ph) + ai * 6.4206383e-9f;
        auto zpow = [&](float e) { const float m = __builtin_amdgcn_exp2f(lr * e); const float t = ph * e; const float f = (t - __builtin_rintf(t)) + pe * e;
                                   return f32x2{m * __builtin_amdgcn_cosf(f), m * __builtin_amdgcn_sinf(f)}; };
        f32x2 z1 = zpow(1.f), Z1 = zpow(256.f); const f32x2 z8 = zpow(8.f);
        z1[1] = -z1[1]; Z1[1] = -Z1[1];
        LAS char* img = lds + L1_KG + w * 8192;
        const int sw = (lane >> 1) & 3;
        auto put8 = [&](int c, const f32x2 (&v)[8]) {
            const u32x4 re = {pk(v[0][0], v[1][0]), pk(v[2][0], v[3][0]), pk(v[4][0], v[5][0]), pk(v[6][0], v[7][0])};
            const u32x4 im = {pk(v[0][1], v[1][1]), pk(v[2][1], v[3][1]), pk(v[4][1], v[5][1]), pk(v[6][1], v[7][1])};
            *(LAS u32x4*)(img + lane * 64 + ((c ^ sw) << 4)) = re;
            *(LAS u32x4*)(img + 4096 + lane * 64 + ((c ^ sw) << 4)) = im; };
        {
            f32x2 v[8];
            v[0] = f32x2{1.f, 0.f}; v[1] = z1; v[2] = cmul(z1, z1); v[3] = cmul(v[2], z1); v[4] = cmul(v[2], v[2]); v[5] = cmul(v[4], z1); v[6] = cmul(v[4], v[2]); v[7] = cmul(v[4], v[3]);
            put8(0, v);
            f32x2 u[8];
            u[0] = Z1; u[1] = cmul(Z1, v[2]); u[2] = cmul(Z1, v[4]); u[3] = cmul(Z1, v[6]); u[4] = cmul(Z1, Z1); u[5] = cmul(u[4], v[4]); u[6] = cmul(u[4], Z1); u[7] = cmul(u[6], v[4]);
            put8(1, u);
            f32x2 t[8];
            t[0] = cmul(u[4], u[4]); t[1] = cmul(t[0], Z1); t[2] = cmul(t[0], u[4]); t[3] = cmul(t[0], u[6]); t[4] = cmul(t[0], t[0]); t[5] = f32x2{0.f, 0.f}; t[6] = t[5]; t[7] = t[5];
            put8(2, t);
        }
        const int G = lane >> 4, q = (lane & 15) >> 2, p = lane & 3, pp = 4 * (G & 1) + p;
        LAS const char* r0 = img + (8 * hh + q) * 64 + (((pp >> 1) ^ (q >> 1)) << 4) + (pp & 1) * 8;
        LAS const char* r1 = img + (8 * hh + 4 + q) * 64 + (((pp >> 1) ^ (2 + (q >> 1))) << 4) + (pp & 1) * 8;
        u32x4 bf[8];
#pragma unroll
        for (int ks = 0; ks < 8; ++ks) bf[ks] = trrd2(r0 + ks * 1024, r1 + ks * 1024);
        const f32x2 cb = cmul(f32x2{bf2f(pCr), bf2f(pCi)}, f32x2{dt * bf2f(pBr), dt * bf2f(pBi)});
        const f32x2 z16 = cmul(z8, z8);
        f32x2 e0 = cb, e1 = cmul(cb, z8);
#pragma unroll
        for (int c = 0; c < 4; ++c) {
            f32x2 v[8];
#pragma unroll
            for (int j = 0; j < 4; ++j) { v[2 * j] = e0; v[2 * j + 1] = e1; if (c < 3 || j < 3) { e0 = cmul(e0, z16); e1 = cmul(e1, z16); } }
            put8(c, v);
        }
        f32x16 kc = {};
#pragma unroll
        for (int ks = 0; ks < 8; ++ks) kc = mfma32(bf[ks], trrd2(r0 + ks * 1024, r1 + ks * 1024), kc);
#pragma unroll
        for (int g = 0; g < 3; ++g) *(LAS f32x4*)(img + r32 * 96 + g * 32 + hh * 16) = f32x4{kc[4 * g], kc[4 * g + 1], kc[4 * g + 2], kc[4 * g + 3]};
    }
    ST(2);
    asm volatile("s_waitcnt vmcnt(0) lgkmcnt(0)\n\ts_barrier"
                 : "+v"(xf[0]), "+v"(xf[1]), "+v"(xf[2]), "+v"(xf[3]), "+v"(xf[4]), "+v"(xf[5]), "+v"(xf[6]), "+v"(xf[7]), "+v"(xf[8]), "+v"(xf[9]), "+v"(xf[10]), "+v"(xf[11]), "+v"(xf[12]), "+v"(xf[13]) :: "memory");
    ST(3);
    f32x16 acc[3] = {};
    {
        const int G = lane >> 4, q = (lane & 15) >> 2, p = lane & 3, p2 = ((p & 1) << 1) | (p >> 1);
        LAS const char* wp = lds + L1_W + (kh * 224 + 8 * hh + q) * 192 + (G & 1) * 32 + p2 * 8;
        if (ct <= 8) gemm_loop<0, 0, 0>(acc, xf, wp); else if (ct == 9) gemm_loop<0, 1, 1>(acc, xf, wp); else gemm_loop<1, 1, 1>(acc, xf, wp);
    }
    ST(4);
    BARRIER();
    {
        LAS char* mine = lds + L1_W + w * 6144 + lane * 16;
        LAS const char* other = lds + L1_W + (w ^ 4) * 6144 + lane * 16;
        float keep[3][8];
#pragma unroll
        for (int k = 0; k < 3; ++k) {
            f32x4 s0, s1;
#pragma unroll
            for (int j = 0; j < 4; ++j) { s0[j] = kh ? acc[k][j] : acc[k][8 + j]; s1[j] = kh ? acc[k][4 + j] : acc[k][12 + j]; }
#pragma unroll
            for (int j = 0; j < 8; ++j) keep[k][j] = kh ? acc[k][8 + j] : acc[k][j];
            *(LAS f32x4*)(mine + k * 2048) = s0; *(LAS f32x4*)(mine + k * 2048 + 1024) = s1;
        }
        BARRIER();
        const int tile = rt * 4 + rb;
#pragma unroll
        for (int k = 0; k < 3; ++k) {
            const f32x4 o0 = *(LAS const f32x4*)(other + k * 2048), o1 = *(LAS const f32x4*)(other + k * 2048 + 1024);
            const int blk = 3 * ct + k;
            const float sc = blk < 14 ? 0.27264374f : 1.f;
            const u32x4 v = {pk((keep[k][0] + o0[0]) * sc, (keep[k][1] + o0[1]) * sc), pk((keep[k][2] + o0[2]) * sc, (keep[k][3] + o0[3]) * sc),
                             pk((keep[k][4] + o1[0]) * sc, (keep[k][5] + o1[1]) * sc), pk((keep[k][6] + o1[2]) * sc, (keep[k][7] + o1[3]) * sc)};
            if (blk < 28) {
                const int part = blk >= 14, gb = (blk - 14 * part) * 4 + 2 * kh;
                *(u32x4*)(ws + (part ? WS_K : WS_Q) + (size_t)(tile * 56 + gb) * 512 + lane * 16) = v;
            } else {
                const int col = (blk - 28) * 32 + ((r32 & 19) | ((r32 & 4) << 1) | ((r32 & 8) >> 1));
                const int hd = col / 28, dd = col - hd * 28, slot = (dd & 19) | ((dd & 4) << 1) | ((dd & 8) >> 1);
                char* dst = ws + WS_V + (size_t)((hd * 64 + tile) * 2 + kh) * 1024 + hh * 512;
                *(u32x4*)(dst + slot * 16) = v;
                if (dd == 27) *(u32x4*)(dst + 28 * 16) = u32x4{0x3f803f80u, 0x3f803f80u, 0x3f803f80u, 0x3f803f80u};
            }
        }
    }
    ST(5);
    {
        const int G = lane >> 4, q = (lane & 15) >> 2, p = lane & 3, rho = w & 3, n0 = 16 * (w >> 2) + 8 * hh;
        LAS const char* up = lds + L1_U + (4 * (n0 + q) + rho) * 64 + (G & 1) * 32 + p * 8;
        const u32x4 v = trrd2(up, up + 16 * 64);
        *(u32x4*)(ws + WS_U + (size_t)(ct * 32 + r32) * 4096 + rt * 256 + rho * 64 + n0 * 2) = v;
    }
    {
        LAS const char* T = lds + L1_KG + (w & 4) * 8192;
        const int i = r32, sec = 2 * wq + hh;
        float o[8];
        if (wq == 0) {
            const int a0 = i * 96 + hh * 32, a1 = hh ? (i < 31 ? (i + 1) * 96 + 32 : 48) : i * 96 + 16;
            f32x4 v = {}, n = {};
#pragma unroll
            for (int k = 0; k < 4; ++k) { v += *(LAS const f32x4*)(T + k * 8192 + a0); n += *(LAS const f32x4*)(T + k * 8192 + a1); }
            o[0] = v[0]; o[1] = hh ? v[0] : v[1]; o[2] = hh ? v[1] : v[2]; o[3] = hh ? v[2] : v[3];
            o[4] = hh ? v[2] : n[0]; o[5] = hh ? v[2] : n[1]; o[6] = hh ? v[3] : n[2]; o[7] = hh ? n[0] : n[3];
            if (lane == 0) o[0] += bf2f(pDs);
        } else if (wq == 1) {
            const int row = 12 + 2 * hh, a0 = i * 96 + row * 4, a1 = i < 31 ? a0 + 96 : (row + 2) * 4;
            f32x2 v = {}; float n = 0.f;
#pragma unroll
            for (int k = 0; k < 4; ++k) { v += *(LAS const f32x2*)(T + k * 8192 + a0); n += *(LAS const float*)(T + k * 8192 + a1); }
            o[0] = o[1] = o[2] = v[0]; o[3] = o[4] = o[5] = v[1]; o[6] = o[7] = n;
        } else {
            const int row = 12 + sec, a0 = i * 96 + row * 4, a1 = i < 31 ? a0 + 96 : (row + 1) * 4;
            float v = 0.f, n = 0.f;
#pragma unroll
            for (int k = 0; k < 4; ++k) { v += *(LAS const float*)(T + k * 8192 + a0); n += *(LAS const float*)(T + k * 8192 + a1); }
            o[0] = o[1] = o[2] = o[3] = v; o[4] = (i & 1) ? n : v; o[5] = o[6] = o[7] = n;
        }
        *(u32x4*)(ws + WS_KX + (size_t)ch * 4096 + (wq * 64 + lane) * 16) = u32x4{pk(o[0], o[1]), pk(o[2], o[3]), pk(o[4], o[5]), pk(o[6], o[7])};
    }
    ST(6);
    ST_FLUSH(0, 7);
}

#define QT 2
constexpr int NKG = 2 * QT;
constexpr int KT = 64 / NKG;
constexpr int L2_KX = 0;
constexpr int L2_U = 8448;
constexpr int L2_R = 20736;
constexpr int L2_L = L2_R + (QT == 2 ? 65536 + 4096 : 8 * QT * 4096);
constexpr int L2_F = L2_L + 8 * QT * 128;
constexpr int L2_BYTES = L2_F + 16;

template <int MODE>
DI void attn_loop(f32x16 (&o)[QT], const u32x4 (&qf)[QT][2], float (&mx)[QT], const char* kp, const char* vp, unsigned lo) {
    u32x4 k0 = *(const u32x4*)(kp + lo), k1 = *(const u32x4*)(kp + lo + 1024);
    u32x4 v0 = *(const u32x4*)(vp + lo), v1 = *(const u32x4*)(vp + lo + 1024);
#pragma unroll 2
    for (int kt = 0; kt < KT; ++kt) {
        const int kn = kt + 1 < KT ? kt + 1 : kt;
        const u32x4 nk0 = *(const u32x4*)(kp + (size_t)kn * 28672 + lo), nk1 = *(const u32x4*)(kp + (size_t)kn * 28672 + lo + 1024);
        const u32x4 nv0 = *(const u32x4*)(vp + (size_t)kn * 2048 + lo), nv1 = *(const u32x4*)(vp + (size_t)kn * 2048 + lo + 1024);
#pragma unroll
        for (int t = 0; t < QT; ++t) {
            f32x16 s = mfma32(k0, qf[t][0], f32x16{});
            s = mfma32(k1, qf[t][1], s);
            if (MODE == 1) {
#pragma unroll
                for (int r = 0; r < 16; ++r) mx[t] = __builtin_fmaxf(mx[t], s[r]);
            } else {
#pragma unroll
                for (int r = 0; r < 16; ++r) s[r] = __builtin_amdgcn_exp2f(MODE == 2 ? s[r] - mx[t] : s[r]);
                const u32x4 p0 = {pk(s[0], s[1]), pk(s[2], s[3]), pk(s[4], s[5]), pk(s[6], s[7])};
                const u32x4 p1 = {pk(s[8], s[9]), pk(s[10], s[11]), pk(s[12], s[13]), pk(s[14], s[15])};
                o[t] = mfma32(v0, p0, o[t]);
                o[t] = mfma32(v1, p1, o[t]);
            }
        }
        k0 = nk0; k1 = nk1; v0 = nv0; v1 = nv1;
    }
}

__global__ __launch_bounds__(512, 2) void k2(char* ws) {
    __shared__ __attribute__((aligned(16))) char lds_[L2_BYTES];
    LAS char* lds = (LAS char*)lds_;
    ST_DECL; ST(0);
    const int tid = threadIdx.x, lane = tid & 63, w = __builtin_amdgcn_readfirstlane(tid >> 6);
    const int b = blockIdx.x, hd = 2 * (b & 7) + ((b >> 3) & 1), rt = b >> 4;
    const int r32 = lane & 31, hh = lane >> 5;
    const int qg = w / NKG, kg = w % NKG;
    const int g0 = (7 * hd) >> 1;
    const bool conv = b < 224;
    if (conv) {
        const int c = w >> 2, t = tid & 255, chn = 2 * b + c;
        const u32x4 kv = *(const u32x4*)(ws + WS_KX + (size_t)chn * 4096 + t * 16);
        const u32x4 uv = *(const u32x4*)(ws + WS_U + (size_t)chn * 4096 + t * 16);
        *(LAS u32x4*)(lds + L2_KX + c * 4224 + 64 + t * 16) = kv;
        if (t < 4) *(LAS u32x4*)(lds + L2_KX + c * 4224 + t * 16) = u32x4{0, 0, 0, 0};
        const int rho = (t >> 2) & 3, chunk = (t >> 4) * 4 + (t & 3);
        *(LAS u32x4*)(lds + L2_U + (c * 4 + rho) * 1536 + 512 + chunk * 16) = uv;
        if (t < 128) *(LAS u32x4*)(lds + L2_U + (c * 4 + (t >> 5)) * 1536 + (t & 31) * 16) = u32x4{0, 0, 0, 0};
    }
    if (tid == 0) *(LAS int*)(lds + L2_F) = 0;
    u32x4 qf[QT][2];
#pragma unroll
    for (int t = 0; t < QT; ++t) {
        const char* qp = ws + WS_Q + (size_t)((rt * 4 + qg * QT + t) * 56 + g0) * 512 + lane * 16;
        qf[t][0] = *(const u32x4*)qp; qf[t][1] = *(const u32x4*)(qp + 1024);
        if (hd & 1) { if (!hh) { qf[t][0][0] = 0; qf[t][0][1] = 0; } } else { if (hh) { qf[t][1][2] = 0; qf[t][1][3] = 0; } }
    }
    const char* kp = ws + WS_K + (size_t)(kg * KT * 56 + g0) * 512;
    const char* vp = ws + WS_V + (size_t)(hd * 64 + kg * KT) * 2048;
    __syncthreads();
    ST(1);
    if (conv) {
        const int c = w >> 2, rho = w & 3;
        const int G = lane >> 4, q = (lane & 15) >> 2, p = lane & 3;
        LAS const char* ap = lds + L2_KX + c * 4224 + (16 - 8 * hh - q + 4 * (G & 1) + p) * 8;
        LAS const char* bp = lds + L2_U + (c * 4 + rho) * 1536 + (32 + r32 - 1 + hh) * 16;
        f32x16 y0 = {}, y1 = {};
#pragma unroll 4
        for (int g = 0; g < 16; ++g) {
            const u32x4 a0 = trrd2(ap + g * 128, ap + g * 128 - 32), a1 = trrd2(ap + g * 128 + 2048, ap + g * 128 + 2048 - 32);
            const u32x4 b0 = *(LAS const u32x4*)(bp - g * 32), b1 = *(LAS const u32x4*)(bp - g * 32 + 512);
            y0 = mfma32(a0, b0, y0); y1 = mfma32(a0, b1, y1); y1 = mfma32(a1, b0, y1);
        }
        LAS char* pl = lds + L2_R + (c * 4 + rho) * 8704 + 16;
#pragma unroll
        for (int g = 0; g < 4; ++g) {
            *(LAS f32x4*)(pl + (r32 * 32 + 8 * g + 4 * hh) * 4) = f32x4{y0[4 * g], y0[4 * g + 1], y0[4 * g + 2], y0[4 * g + 3]};
            *(LAS f32x4*)(pl + 4096 + (r32 * 32 + 8 * g + 4 * hh) * 4) = f32x4{y1[4 * g], y1[4 * g + 1], y1[4 * g + 2], y1[4 * g + 3]};
        }
        if (lane == 0) *(LAS f32x4*)(pl - 16) = f32x4{0.f, 0.f, 0.f, 0.f};
        __syncthreads();
        const int t = tid & 255;
        LAS const char* rp = lds + L2_R + (c * 4) * 8704 + t * 32;
        float y[8];
        { const f32x4 m = *(LAS const f32x4*)(rp + 16), n = *(LAS const f32x4*)(rp + 32);
#pragma unroll
          for (int j = 0; j < 4; ++j) { y[j] = m[j]; y[4 + j] = n[j]; } }
#pragma unroll
        for (int r = 1; r < 4; ++r) {
            const f32x4 l = *(LAS const f32x4*)(rp + r * 8704), m = *(LAS const f32x4*)(rp + r * 8704 + 16), n = *(LAS const f32x4*)(rp + r * 8704 + 32);
            float e[12];
#pragma unroll
            for (int j = 0; j < 4; ++j) { e[j] = l[j]; e[4 + j] = m[j]; e[8 + j] = n[j]; }
#pragma unroll
            for (int j = 0; j < 8; ++j) y[j] += e[4 + j - r];
        }
        *(u32x4*)(ws + WS_Y + (size_t)(2 * b + c) * 4096 + t * 16) = u32x4{pk(y[0], y[1]), pk(y[2], y[3]), pk(y[4], y[5]), pk(y[6], y[7])};
        __syncthreads();
    }
    ST(2);
    f32x16 o[QT]; float mx[QT];
#pragma unroll
    for (int t = 0; t < QT; ++t) { o[t] = f32x16{}; mx[t] = 0.f; }
    attn_loop<0>(o, qf, mx, kp, vp, lane * 16);
    ST(3);
    bool done = false;
    for (int pass = 0; pass < 2; ++pass) {
        LAS char* wr = lds + L2_R + w * (QT * 4096) + lane * 16;
#pragma unroll
        for (int t = 0; t < QT; ++t) {
#pragma unroll
            for (int g = 0; g < 4; ++g) *(LAS f32x4*)(wr + (t * 4 + g) * 1024) = f32x4{o[t][4 * g], o[t][4 * g + 1], o[t][4 * g + 2], o[t][4 * g + 3]};
        }
        __syncthreads();
        const int ut = kg >> 1, us = kg & 1;
        LAS const char* rdp = lds + L2_R + qg * NKG * (QT * 4096) + ut * 4096;
        f32x4 a0 = {}, a1 = {}; float l = 0.f;
#pragma unroll
        for (int k = 0; k < NKG; ++k) {
            a0 += *(LAS const f32x4*)(rdp + k * (QT * 4096) + us * 2048 + lane * 16);
            a1 += *(LAS const f32x4*)(rdp + k * (QT * 4096) + us * 2048 + 1024 + lane * 16);
            l += *(LAS const float*)(rdp + k * (QT * 4096) + 3072 + (32 + r32) * 16);
        }
        if (us && hh) a1 = f32x4{0.f, 0.f, 0.f, 0.f};
        const float am = __builtin_fmaxf(__builtin_fmaxf(__builtin_fmaxf(__builtin_fabsf(a0[0]), __builtin_fabsf(a0[1])), __builtin_fmaxf(__builtin_fabsf(a0[2]), __builtin_fabsf(a0[3]))),
                                         __builtin_fmaxf(__builtin_fmaxf(__builtin_fabsf(a1[0]), __builtin_fabsf(a1[1])), __builtin_fmaxf(__builtin_fabsf(a1[2]), __builtin_fabsf(a1[3]))));
        const float chk = (a0[0] + a0[1] + a0[2] + a0[3] + a1[0] + a1[1] + a1[2] + a1[3]) * 0.f;
        const bool good = l >= 0x1p-20f && l <= 0x1p80f && am <= 0x1p120f && chk == 0.f;
        const bool allgood = __all(good) || pass == 1;
        if (allgood && !done) {
            const float rl = 1.f / l;
            const u32x4 v = {pk(a0[0] * rl, a0[1] * rl), pk(a0[2] * rl, a0[3] * rl), pk(a1[0] * rl, a1[1] * rl), pk(a1[2] * rl, a1[3] * rl)};
            *(u32x4*)(ws + WS_O + (size_t)(((rt * 4 + qg * QT + ut) * 16 + hd) * 4 + 2 * us) * 512 + lane * 16) = v;
            done = true;
        }
        if (pass == 1) break;
        if (!allgood && lane == 0) *(LAS int*)(lds + L2_F) = 1;
        __syncthreads();
        if (*(LAS volatile int*)(lds + L2_F) == 0) break;
#pragma unroll
        for (int t = 0; t < QT; ++t) { o[t] = f32x16{}; mx[t] = -__builtin_inff(); }
        attn_loop<1>(o, qf, mx, kp, vp, lane * 16);
#pragma unroll
        for (int t = 0; t < QT; ++t) { mx[t] = __builtin_fmaxf(mx[t], __shfl_xor(mx[t], 32)); if (!hh) *(LAS float*)(lds + L2_L + ((w * QT + t) * 32 + r32) * 4) = mx[t]; }
        __syncthreads();
#pragma unroll
        for (int t = 0; t < QT; ++t)
            for (int k = 0; k < NKG; ++k) mx[t] = __builtin_fmaxf(mx[t], *(LAS const float*)(lds + L2_L + (((qg * NKG + k) * QT + t) * 32 + r32) * 4));
        attn_loop<2>(o, qf, mx, kp, vp, lane * 16);
    }
    ST(4);
    ST_FLUSH(7, 5);
}

constexpr int L3_W = 0;
constexpr int L3_O = 101376;
constexpr int L3_Y = L3_O + 32768;
constexpr int L3_G = L3_Y + 7168;
constexpr int L3_BYTES = L3_G + 128;

__global__ __launch_bounds__(512, 2) void k3(const u16* x, const u16* wo, const u16* gw, char* ws, u16* out) {
    __shared__ __attribute__((aligned(16))) char lds_[L3_BYTES];
    LAS char* lds = (LAS char*)lds_;
    ST_DECL; ST(0);
    const unsigned lds0 = (unsigned)(uintptr_t)lds;
    const int tid = threadIdx.x, lane = tid & 63, w = __builtin_amdgcn_readfirstlane(tid >> 6);
    const int b = blockIdx.x, xcd = b & 7, bi = b >> 3;
    const int ct = bi & 3, rt = xcd * 8 + (bi >> 2);
    for (int i = w; i < 98; i += 8) {
        const unsigned o = (i % 7) * 1024 + lane * 16;
        dma16((o / 224) * 896 + (o % 224), (const char*)wo + (size_t)(i / 7) * 32 * 896 + ct * 224, lds0 + L3_W + i * 1024);
    }
#pragma unroll
    for (int i = 0; i < 4; ++i) dma16(lane * 16, ws + WS_O + (size_t)rt * 32768 + (w * 4 + i) * 1024, lds0 + L3_O + (w * 4 + i) * 1024);
    if (w < 7) dma16((16 * w + (lane >> 2)) * 4096 + (lane & 3) * 16, ws + WS_Y + (size_t)ct * 112 * 4096 + rt * 64, lds0 + L3_Y + w * 1024);
    if (tid < 56) *(LAS u32x4*)(lds + L3_W + 448 * 224 + tid * 16) = u32x4{0, 0, 0, 0};
    const int r16 = lane & 15, c4 = lane >> 4;
    if (w == 7) {
        f32x4 z0 = {}, z1 = {};
        const u16* xp = x + (size_t)(rt * 32 + r16) * D + c4 * 8;
#pragma unroll
        for (int ks = 0; ks < 14; ++ks) {
            const u32x4 gf = *(const u32x4*)(gw + ks * 32 + c4 * 8);
            z0 = mfma16(gf, *(const u32x4*)(xp + ks * 32), z0);
            z1 = mfma16(gf, *(const u32x4*)(xp + 16 * D + ks * 32), z1);
        }
        if (lane < 32) { const float z = lane < 16 ? z0[0] : z1[0]; *(LAS float*)(lds + L3_G + lane * 4) = 1.f / (1.f + __expf(-z)); }
    }
    asm volatile("s_waitcnt vmcnt(0) lgkmcnt(0)\n\ts_barrier" ::: "memory");
    ST(1);
    if (w < 7) {
        const int q = r16 >> 2, p = lane & 3;
        LAS const char* wp = lds + L3_W + (8 * c4 + q) * 224 + w * 32 + p * 8;
        LAS const char* op = lds + L3_O + c4 * 512 + r16 * 16;
        f32x4 c0 = {}, c1 = {};
#pragma unroll
        for (int hd = 0; hd < 16; ++hd) {
            const u32x4 a = trrd2(wp + hd * 28 * 224, wp + hd * 28 * 224 + 4 * 224);
            c0 = mfma16(a, *(LAS const u32x4*)(op + hd * 2048), c0);
            c1 = mfma16(a, *(LAS const u32x4*)(op + hd * 2048 + 256), c1);
        }
        LAS const char* yp = lds + L3_Y + (16 * w + 4 * c4 + q) * 64 + p * 8;
        const u32x2 y0 = trrd(yp), y1 = trrd(yp + 32);
        const float g0 = *(LAS const float*)(lds + L3_G + r16 * 4), g1 = *(LAS const float*)(lds + L3_G + 64 + r16 * 4);
        auto mix = [](float g, float a, float bb) { return g * a + (1.f - g) * bb; };
        u16* op0 = out + (size_t)(rt * 32 + r16) * D + ct * 112 + w * 16 + c4 * 4;
        *(u32x2*)op0 = u32x2{pk(mix(g0, c0[0], bf2f(y0[0] & 0xffffu)), mix(g0, c0[1], __uint_as_float(y0[0] & 0xffff0000u))),
                             pk(mix(g0, c0[2], bf2f(y0[1] & 0xffffu)), mix(g0, c0[3], __uint_as_float(y0[1] & 0xffff0000u)))};
        *(u32x2*)(op0 + 16 * D) = u32x2{pk(mix(g1, c1[0], bf2f(y1[0] & 0xffffu)), mix(g1, c1[1], __uint_as_float(y1[0] & 0xffff0000u))),
                                        pk(mix(g1, c1[2], bf2f(y1[1] & 0xffffu)), mix(g1, c1[3], __uint_as_float(y1[1] & 0xffff0000u)))};
    }
    ST(2);
    ST_FLUSH(12, 3);
}

__global__ void kempty(char* ws) { if (ws == nullptr) ws[0] = 1; }
extern "C" void kernel_launch(void* const* d_in, const int* in_sizes, int n_in, void* d_out, int out_size, void* d_ws, size_t ws_size, hipStream_t stream) {
 const u16* x = (const u16*)d_in[0]; char* ws = (char*)d_ws;
kempty<<<256, 512, 0, stream>>>(ws);
k1<<<224, 512, 0, stream>>>(x, (const u16*)d_in[1], (const u16*)d_in[4], (const u16*)d_in[5], (const u16*)d_in[6], (const u16*)d_in[7], (const u16*)d_in[8], (const u16*)d_in[9], (const u16*)d_in[10], (const u16*)d_in[11], ws);
k2<<<256, 512, 0, stream>>>(ws);
k3<<<256, 512, 0, stream>>>(x, (const u16*)d_in[2], (const u16*)d_in[12], ws, (u16*)d_out);
}
```

```cpp
#include <hip/hip_runtime.h>
#include <hip/hip_bf16.h>
#include <stdint.h>

typedef unsigned short u16;
typedef unsigned long long u64;
typedef __attribute__((ext_vector_type(8))) __bf16 bf16x8;
typedef __attribute__((ext_vector_type(2))) __bf16 bf16x2;
typedef __attribute__((ext_vector_type(4))) short s16x4;
typedef __attribute__((ext_vector_type(2))) float f32x2;
typedef __attribute__((ext_vector_type(4))) float f32x4;
typedef __attribute__((ext_vector_type(16))) float f32x16;
typedef __attribute__((ext_vector_type(2))) unsigned u32x2;
typedef __attribute__((ext_vector_type(4))) unsigned u32x4;
typedef __attribute__((ext_vector_type(8))) unsigned u32x8;
typedef __attribute__((ext_vector_type(16))) unsigned u32x16;
#define LAS __attribute__((address_space(3)))
#define GAS __attribute__((address_space(1)))
#define DI __device__ __forceinline__

constexpr int D = 448;
constexpr unsigned WS_W = 0;
constexpr unsigned RS = 33280;
constexpr unsigned WS_K = 1u << 20;
constexpr unsigned WS_V = WS_K + 64 * RS;
constexpr unsigned WS_U = 6u << 20;
constexpr unsigned WS_O = 8u << 20;
constexpr unsigned WS_Y = 10u << 20;
constexpr unsigned WS_G = 12u << 20;
constexpr unsigned WS_FU = WS_G + 8192;
constexpr unsigned WS_F1 = WS_FU + 32768;
constexpr unsigned WS_FH = WS_F1 + 8192;
constexpr unsigned WS_FW = WS_F1 + 24576;
constexpr unsigned WS_F2 = WS_F1 + 32768;
constexpr unsigned WS_FM = WS_F2 + 4096;
constexpr unsigned WS_F3 = WS_FM + 16384;
constexpr unsigned WS_FY = WS_F3 + 8192;
constexpr unsigned WS_E = WS_FY + 8192;
constexpr unsigned WS_END = WS_E + 4096;
#define A_ASM \
    "s_mov_b32 %[m0s], m0\n" \
    "v_mov_b32 v208, %[eo]\n" \
    "global_load_dwordx2 %[tag], v208, s[62:63] sc1\n" \
    "s_mov_b32 %[cnt], %[off]\n" \
    "s_add_u32 %[cnt], %[cnt], 128\n" \
    "s_cmp_eq_u32 %[cnt], 896\n" \
    "s_cselect_b32 %[cnt], 0, %[cnt]\n" \
    "s_mul_i32 s50, %[cnt], 1344\n" \
    "s_add_u32 s50, s78, s50\n" \
    "s_addc_u32 s51, s79, 0\n" \
    "s_add_u32 s36, s76, %[cnt]\n" \
    "s_addc_u32 s37, s77, 0\n" \
    "s_add_u32 m0, %[wdst], 8192\n" \
    "s_nop 0\n" \
    "global_load_lds_dwordx4 %[vo0], s[50:51] offset:0\n" \
    "s_add_u32 %[cnt], %[cnt], 128\n" \
    "s_cmp_eq_u32 %[cnt], 896\n" \
    "s_cselect_b32 %[cnt], 0, %[cnt]\n" \
    "s_mul_i32 s52, %[cnt], 1344\n" \
    "s_add_u32 s52, s78, s52\n" \
    "s_addc_u32 s53, s79, 0\n" \
    "s_add_u32 s38, s76, %[cnt]\n" \
    "s_addc_u32 s39, s77, 0\n" \
    "s_add_u32 %[cnt], %[cnt], 128\n" \
    "s_cmp_eq_u32 %[cnt], 896\n" \
    "s_cselect_b32 %[cnt], 0, %[cnt]\n" \
    "s_mul_i32 s54, %[cnt], 1344\n" \
    "s_add_u32 s54, s78, s54\n" \
    "s_addc_u32 s55, s79, 0\n" \
    "s_add_u32 s40, s76, %[cnt]\n" \
    "s_addc_u32 s41, s77, 0\n" \
    "s_add_u32 %[cnt], %[cnt], 128\n" \
    "s_cmp_eq_u32 %[cnt], 896\n" \
    "s_cselect_b32 %[cnt], 0, %[cnt]\n" \
    "s_mul_i32 s56, %[cnt], 1344\n" \
    "s_add_u32 s56, s78, s56\n" \
    "s_addc_u32 s57, s79, 0\n" \
    "s_add_u32 s42, s76, %[cnt]\n" \
    "s_addc_u32 s43, s77, 0\n" \
    "global_load_lds_dwordx4 %[vo1], s[50:51] offset:1024\n" \
    "s_add_u32 %[cnt], %[cnt], 128\n" \
    "s_cmp_eq_u32 %[cnt], 896\n" \
    "s_cselect_b32 %[cnt], 0, %[cnt]\n" \
    "s_mul_i32 s58, %[cnt], 1344\n" \
    "s_add_u32 s58, s78, s58\n" \
    "s_addc_u32 s59, s79, 0\n" \
    "s_add_u32 s44, s76, %[cnt]\n" \
    "s_addc_u32 s45, s77, 0\n" \
    "s_add_u32 %[cnt], %[cnt], 128\n" \
    "s_cmp_eq_u32 %[cnt], 896\n" \
    "s_cselect_b32 %[cnt], 0, %[cnt]\n" \
    "s_mul_i32 s60, %[cnt], 1344\n" \
    "s_add_u32 s60, s78, s60\n" \
    "s_addc_u32 s61, s79, 0\n" \
    "s_add_u32 s46, s76, %[cnt]\n" \
    "s_addc_u32 s47, s77, 0\n" \
    "s_mul_i32 s48, %[off], 1344\n" \
    "s_add_u32 s48, s78, s48\n" \
    "s_addc_u32 s49, s79, 0\n" \
    "s_add_u32 s34, s62, 66560\n" \
    "s_addc_u32 s35, s63, 0\n" \
    "v_and_b32 v176, 31, %[ln]\n" \
    "s_add_u32 m0, %[lent], 0\n" \
    "s_nop 0\n" \
    "global_load_lds_dwordx4 %[xo0], s[36:37] offset:0\n" \
    "v_lshrrev_b32 v177, 5, %[ln]\n" \
    "v_lshlrev_b32 v178, 4, %[ln]\n" \
    "v_lshlrev_b32 v179, 4, v176\n" \
    "v_bfe_u32 v183, %[ln], 1, 3\n" \
    "v_xor_b32 v183, v183, v177\n" \
    "v_lshlrev_b32 v184, 7, v176\n" \
    "v_lshl_or_b32 v183, v183, 4, v184\n" \
    "v_add_u32 v212, %[ring], v183\n" \
    "v_add_u32 v216, %[lent], v183\n" \
    "v_xor_b32 v213, 32, v212\n" \
    "v_xor_b32 v217, 32, v216\n" \
    "v_xor_b32 v214, 64, v212\n" \
    "v_xor_b32 v218, 64, v216\n" \
    "v_xor_b32 v215, 96, v212\n" \
    "v_xor_b32 v219, 96, v216\n" \
    "v_mov_b32 v232, 159744\n" \
    "v_bfe_u32 v180, %[ln], 2, 2\n" \
    "v_and_b32 v181, 3, %[ln]\n" \
    "v_and_b32 v182, 1, %[ln]\n" \
    "v_bfe_u32 v183, %[ln], 1, 1\n" \
    "v_lshl_or_b32 v182, v182, 1, v183\n" \
    "v_bfe_u32 v183, %[ln], 4, 1\n" \
    "global_load_lds_dwordx4 %[xo1], s[36:37] offset:1024\n" \
    "v_lshl_or_b32 v182, v183, 2, v182\n" \
    "v_lshlrev_b32 v182, 3, v182\n" \
    "v_lshl_or_b32 v183, v177, 3, v180\n" \
    "v_lshl_or_b32 v184, v183, 7, v182\n" \
    "v_bfe_u32 v185, %[ln], 3, 1\n" \
    "v_lshl_or_b32 v220, v185, 6, v184\n" \
    "v_xor_b32 v221, 64, v220\n" \
    "v_lshl_or_b32 v184, v183, 6, v182\n" \
    "v_add_u32 v226, 57344, v184\n" \
    "s_mul_i32 s88, %[w], 43008\n" \
    "s_lshl_b32 s89, %[g0], 4\n" \
    "s_add_u32 s88, s88, s89\n" \
    "v_lshrrev_b32 v183, 2, %[ln]\n" \
    "v_mul_u32_u24 v183, 2688, v183\n" \
    "v_lshl_add_u32 v183, v181, 4, v183\n" \
    "v_add_u32 v225, s88, v183\n" \
    "s_lshr_b32 s88, %[g0], 2\n" \
    "s_add_u32 s89, %[g0], 3\n" \
    "s_lshr_b32 s89, s89, 2\n" \
    "s_lshl_b32 s89, s89, 8\n" \
    "s_or_b32 s88, s88, s89\n" \
    "s_add_u32 s89, %[hd], 16\n" \
    "global_load_lds_dwordx4 %[xo2], s[36:37] offset:2048\n" \
    "s_mul_i32 s89, s89, 0x1010000\n" \
    "s_or_b32 s88, s88, s89\n" \
    "v_and_b32 v183, 24, %[ln]\n" \
    "v_lshrrev_b32 v184, v183, s88\n" \
    "v_and_b32 v184, 255, v184\n" \
    "v_lshl_add_u32 v184, v184, 2, %[w]\n" \
    "s_lshl_b32 s89, %[hj], 6\n" \
    "v_bfe_u32 v185, %[ln], 3, 2\n" \
    "v_mul_u32_u24 v186, 21, v185\n" \
    "v_add_u32 v186, s89, v186\n" \
    "v_mul_u32_u24 v186, 2341, v186\n" \
    "v_lshrrev_b32 v186, 16, v186\n" \
    "v_add_u32 v186, 16, v186\n" \
    "s_lshl_b32 s90, %[hj], 1\n" \
    "v_and_or_b32 v185, v185, 1, s90\n" \
    "s_cmp_eq_u32 %[e], 0\n" \
    "s_cselect_b64 s[90:91], 0, -1\n" \
    "s_nop 0\n" \
    "v_cndmask_b32 v185, v185, v186, s[90:91]\n" \
    "s_lshr_b32 s88, %[rt], 2\n" \
    "v_lshl_add_u32 v185, v185, 2, s88\n" \
    "s_and_b32 s88, %[rt], 3\n" \
    "global_load_lds_dwordx4 %[xo3], s[36:37] offset:3072\n" \
    "s_add_u32 s83, %[hj], 1\n" \
    "s_lshr_b32 s83, s83, 3\n" \
    "s_or_b32 s88, s88, s83\n" \
    "s_cselect_b32 vcc_hi, 0, -1\n" \
    "s_mov_b32 vcc_lo, 0\n" \
    "s_nop 0\n" \
    "v_cndmask_b32 v184, v184, v185, vcc\n" \
    "v_lshlrev_b32 v184, 7, v184\n" \
    "v_add_u32 v224, 12660736, v184\n" \
    "v_and_b32 v183, 19, v176\n" \
    "v_bfe_u32 v184, v176, 2, 1\n" \
    "v_lshl_or_b32 v183, v184, 3, v183\n" \
    "v_bfe_u32 v184, v176, 3, 1\n" \
    "v_lshl_or_b32 v183, v184, 2, v183\n" \
    "v_add_u32 v183, s89, v183\n" \
    "s_lshl_b32 s88, %[rt], 2\n" \
    "s_add_u32 s88, s88, %[w]\n" \
    "s_lshl_b32 s89, s88, 9\n" \
    "s_add_u32 s89, s89, 1048576\n" \
    "s_lshl_b32 s83, %[hj], 3\n" \
    "v_mul_u32_u24 v184, 2341, v183\n" \
    "v_lshrrev_b32 v184, 16, v184\n" \
    "s_add_u32 m0, %[wdst], 16384\n" \
    "s_nop 0\n" \
    "global_load_lds_dwordx4 %[vo0], s[52:53] offset:0\n" \
    "v_mul_u32_u24 v185, 28, v184\n" \
    "v_sub_u32 v185, v183, v185\n" \
    "v_lshl_add_u32 v184, v184, 2, 64\n" \
    "v_mov_b32 v186, s83\n" \
    "v_cndmask_b32 v184, v186, v184, s[90:91]\n" \
    "v_add_u32 v184, v184, v177\n" \
    "v_and_b32 v186, 19, v185\n" \
    "v_bfe_u32 v187, v185, 2, 1\n" \
    "v_lshl_or_b32 v186, v187, 3, v186\n" \
    "v_bfe_u32 v187, v185, 3, 1\n" \
    "v_lshl_or_b32 v186, v187, 2, v186\n" \
    "v_cndmask_b32 v186, v176, v186, s[90:91]\n" \
    "v_mul_u32_u24 v184, 33280, v184\n" \
    "v_lshl_add_u32 v184, v186, 4, v184\n" \
    "v_add_u32 v222, s89, v184\n" \
    "v_add_u32 v183, 32, v183\n" \
    "s_add_u32 s83, s83, 4\n" \
    "v_mul_u32_u24 v184, 2341, v183\n" \
    "v_lshrrev_b32 v184, 16, v184\n" \
    "v_mul_u32_u24 v185, 28, v184\n" \
    "v_sub_u32 v185, v183, v185\n" \
    "v_lshl_add_u32 v184, v184, 2, 64\n" \
    "global_load_lds_dwordx4 %[vo1], s[52:53] offset:1024\n" \
    "v_mov_b32 v186, s83\n" \
    "v_cndmask_b32 v184, v186, v184, s[90:91]\n" \
    "v_add_u32 v184, v184, v177\n" \
    "v_and_b32 v186, 19, v185\n" \
    "v_bfe_u32 v187, v185, 2, 1\n" \
    "v_lshl_or_b32 v186, v187, 3, v186\n" \
    "v_bfe_u32 v187, v185, 3, 1\n" \
    "v_lshl_or_b32 v186, v187, 2, v186\n" \
    "v_cndmask_b32 v186, v176, v186, s[90:91]\n" \
    "v_mul_u32_u24 v184, 33280, v184\n" \
    "v_lshl_add_u32 v184, v186, 4, v184\n" \
    "v_add_u32 v223, s89, v184\n" \
    "s_lshl_b32 s89, s88, 4\n" \
    "s_add_u32 s89, s89, %[hd]\n" \
    "s_lshl_b32 s81, s89, 3\n" \
    "s_add_u32 s81, s81, 12677120\n" \
    "s_lshl_b32 s89, s89, 11\n" \
    "s_add_u32 s89, s89, 8388608\n" \
    "v_add_u32 v234, s89, v178\n" \
    "s_lshl_b32 s89, %[frt], 7\n" \
    "s_add_u32 s89, s89, 12677120\n" \
    "v_and_b32 v183, 15, %[ln]\n" \
    "s_add_u32 m0, %[ring], 8192\n" \
    "s_nop 0\n" \
    "global_load_lds_dwordx4 %[xo0], s[38:39] offset:0\n" \
    "v_lshl_add_u32 v240, v183, 3, s89\n" \
    "v_mul_u32_u24 v183, 33280, v177\n" \
    "v_add_u32 v183, v183, v179\n" \
    "s_lshl_b32 s88, %[w], 13\n" \
    "s_mul_i32 s89, %[g0], 33280\n" \
    "s_add_u32 s89, s89, s88\n" \
    "s_add_u32 s89, s89, 1048576\n" \
    "v_add_u32 v236, s89, v183\n" \
    "s_mul_i32 s89, %[hd], 133120\n" \
    "s_add_u32 s89, s89, s88\n" \
    "s_add_u32 s89, s89, 3178496\n" \
    "v_add_u32 v237, s89, v183\n" \
    "s_lshr_b32 s88, %[g0], 3\n" \
    "s_add_u32 s89, %[g0], 3\n" \
    "s_lshr_b32 s89, s89, 3\n" \
    "s_lshl_b32 s89, s89, 8\n" \
    "s_or_b32 s88, s88, s89\n" \
    "s_mul_i32 s83, %[hd], 28\n" \
    "s_lshr_b32 s89, s83, 6\n" \
    "s_lshl_b32 s89, s89, 16\n" \
    "s_or_b32 s88, s88, s89\n" \
    "s_add_u32 s83, s83, 27\n" \
    "global_load_lds_dwordx4 %[xo1], s[38:39] offset:1024\n" \
    "s_lshr_b32 s89, s83, 6\n" \
    "s_lshl_b32 s89, s89, 24\n" \
    "s_or_b32 s88, s88, s89\n" \
    "s_add_u32 s88, s88, 0x7070000\n" \
    "v_lshrrev_b32 v183, 4, %[ln]\n" \
    "v_lshlrev_b32 v183, 3, v183\n" \
    "v_lshrrev_b32 v183, v183, s88\n" \
    "v_and_b32 v183, 255, v183\n" \
    "v_lshlrev_b32 v183, 9, v183\n" \
    "v_and_b32 v184, 15, %[ln]\n" \
    "v_lshl_add_u32 v183, v184, 3, v183\n" \
    "s_lshl_b32 s89, %[w], 7\n" \
    "s_add_u32 s88, s89, 12623872\n" \
    "v_add_u32 v238, s88, v183\n" \
    "s_lshl_b32 s88, %[rt], 1\n" \
    "s_add_u32 s88, s88, %[e]\n" \
    "s_lshl_b32 s88, s88, 9\n" \
    "s_add_u32 s88, s88, s89\n" \
    "s_add_u32 s80, s88, 12632064\n" \
    "s_sub_u32 s88, %[hd], %[e]\n" \
    "s_lshl_b32 s88, s88, 4\n" \
    "s_add_u32 s88, s88, %[rt]\n" \
    "global_load_lds_dwordx4 %[xo2], s[38:39] offset:2048\n" \
    "s_lshl_b32 s88, s88, 2\n" \
    "s_add_u32 s88, s88, %[w]\n" \
    "s_lshl_b32 s88, s88, 3\n" \
    "s_add_u32 s82, s88, 12623872\n" \
    "s_and_b32 s88, %[hd], 1\n" \
    "s_cselect_b32 s84, -1, 0\n" \
    "s_cselect_b32 s87, 0, -1\n" \
    "s_mov_b32 s85, 0\n" \
    "s_mov_b32 s86, 0\n" \
    "s_lshl_b32 s88, %[w], 11\n" \
    "v_add_u32 v227, s88, v178\n" \
    "s_sub_u32 s88, %[ring], 40960\n" \
    "v_add_u32 v233, s88, v178\n" \
    "s_add_u32 s88, %[w], 1\n" \
    "s_and_b32 s88, s88, 3\n" \
    "v_lshl_add_u32 v228, s88, 11, v178\n" \
    "s_mul_i32 s88, s88, 12288\n" \
    "s_add_u32 s88, s88, 16384\n" \
    "v_add_u32 v229, s88, v178\n" \
    "s_add_u32 s88, %[w], 2\n" \
    "s_and_b32 s88, s88, 3\n" \
    "v_lshl_add_u32 v239, s88, 11, v178\n" \
    "global_load_lds_dwordx4 %[xo3], s[38:39] offset:3072\n" \
    "s_mul_i32 s88, s88, 12288\n" \
    "s_add_u32 s88, s88, 20480\n" \
    "v_add_u32 v230, s88, v178\n" \
    "s_add_u32 s88, %[w], 3\n" \
    "s_and_b32 s88, s88, 3\n" \
    "v_lshl_add_u32 v235, s88, 11, v178\n" \
    "s_mul_i32 s88, s88, 12288\n" \
    "s_add_u32 s88, s88, 24576\n" \
    "v_add_u32 v231, s88, v178\n" \
    "s_cmp_eq_u32 %[mode], 1\n" \
    "s_cbranch_scc1 LM1_%=\n" \
    "s_cmp_eq_u32 %[mode], 0\n" \
    "s_cbranch_scc0 LM2_%=\n" \
    "s_waitcnt vmcnt(15)\n" \
    "ds_read_b128 v[0:3], v212 offset:4096\n" \
    "ds_read_b128 v[4:7], v213 offset:4096\n" \
    "ds_read_b128 v[8:11], v214 offset:4096\n" \
    "ds_read_b128 v[12:15], v215 offset:4096\n" \
    "s_waitcnt vmcnt(12)\n" \
    "v_lshl_add_u64 %[tag], %[tag], 0, %[tc]\n" \
    "s_waitcnt vmcnt(10)\n" \
    "v_mov_b32 v113, 1\n" \
    "s_mov_b64 exec, 1\n" \
    "ds_add_u32 v232, v113 offset:16\n" \
    "s_mov_b64 exec, -1\n" \
    "s_add_u32 m0, %[wdst], 24576\n" \
    "s_nop 0\n" \
    "global_load_lds_dwordx4 %[vo0], s[54:55] offset:0\n" \
    "global_load_lds_dwordx4 %[vo1], s[54:55] offset:1024\n" \
    "s_mov_b32 %[cnt], 0x200000\n" \
    "LSPW0_%=:\n" \
    "ds_read_b32 v112, v232 offset:16\n" \
    "s_sub_u32 %[cnt], %[cnt], 1\n" \
    "s_cbranch_scc1 LGOW0_%=\n" \
    "s_waitcnt lgkmcnt(0)\n" \
    "v_cmp_gt_u32 vcc, 4, v112\n" \
    "s_cbranch_vccnz LSPW0_%=\n" \
    "LGOW0_%=:\n" \
    "s_waitcnt lgkmcnt(0)\n" \
    "ds_read_b64_tr_b16 v[112:113], v220 offset:0\n" \
    "ds_read_b64_tr_b16 v[114:115], v220 offset:512\n" \
    "ds_read_b64_tr_b16 v[116:117], v221 offset:0\n" \
    "ds_read_b64_tr_b16 v[118:119], v221 offset:512\n" \
    "ds_read_b64_tr_b16 v[120:121], v220 offset:2048\n" \
    "ds_read_b64_tr_b16 v[122:123], v220 offset:2560\n" \
    "ds_read_b64_tr_b16 v[124:125], v221 offset:2048\n" \
    "ds_read_b64_tr_b16 v[126:127], v221 offset:2560\n" \
    "ds_read_b64_tr_b16 v[128:129], v220 offset:4096\n" \
    "ds_read_b64_tr_b16 v[130:131], v220 offset:4608\n" \
    "ds_read_b64_tr_b16 v[132:133], v221 offset:4096\n" \
    "ds_read_b64_tr_b16 v[134:135], v221 offset:4608\n" \
    "ds_read_b64_tr_b16 v[136:137], v220 offset:6144\n" \
    "ds_read_b64_tr_b16 v[138:139], v220 offset:6656\n" \
    "ds_read_b64_tr_b16 v[140:141], v221 offset:6144\n" \
    "ds_read_b64_tr_b16 v[142:143], v221 offset:6656\n" \
    "v_mov_b32 v210, 1\n" \
    "s_add_u32 m0, %[wdst], 32768\n" \
    "s_nop 0\n" \
    "global_load_lds_dwordx4 %[vo0], s[56:57] offset:0\n" \
    "s_waitcnt lgkmcnt(14)\n" \
    "v_mfma_f32_32x32x16_bf16 v[176:191], v[112:115], v[0:3], 0\n" \
    "ds_read_b64_tr_b16 v[144:145], v220 offset:8192\n" \
    "ds_read_b64_tr_b16 v[146:147], v220 offset:8704\n" \
    "global_load_lds_dwordx4 %[vo1], s[56:57] offset:1024\n" \
    "s_waitcnt lgkmcnt(14)\n" \
    "v_mfma_f32_32x32x16_bf16 v[192:207], v[116:119], v[0:3], 0\n" \
    "ds_read_b64_tr_b16 v[148:149], v221 offset:8192\n" \
    "ds_read_b64_tr_b16 v[150:151], v221 offset:8704\n" \
    "s_add_u32 m0, %[ring], 0\n" \
    "s_nop 0\n" \
    "global_load_lds_dwordx4 %[xo0], s[40:41] offset:0\n" \
    "s_waitcnt lgkmcnt(14)\n" \
    "v_mfma_f32_32x32x16_bf16 v[176:191], v[120:123], v[4:7], v[176:191]\n" \
    "ds_read_b64_tr_b16 v[152:153], v220 offset:10240\n" \
    "ds_read_b64_tr_b16 v[154:155], v220 offset:10752\n" \
    "global_load_lds_dwordx4 %[xo1], s[40:41] offset:1024\n" \
    "s_waitcnt lgkmcnt(14)\n" \
    "v_mfma_f32_32x32x16_bf16 v[192:207], v[124:127], v[4:7], v[192:207]\n" \
    "ds_read_b64_tr_b16 v[156:157], v221 offset:10240\n" \
    "ds_read_b64_tr_b16 v[158:159], v221 offset:10752\n" \
    "s_waitcnt lgkmcnt(14)\n" \
    "v_mfma_f32_32x32x16_bf16 v[176:191], v[128:131], v[8:11], v[176:191]\n" \
    "ds_read_b64_tr_b16 v[160:161], v220 offset:12288\n" \
    "ds_read_b64_tr_b16 v[162:163], v220 offset:12800\n" \
    "s_waitcnt vmcnt(10)\n" \
    "s_mov_b64 exec, 1\n" \
    "ds_add_u32 v232, v210 offset:44\n" \
    "s_mov_b64 exec, -1\n" \
    "ds_read_b128 v[16:19], v216 offset:0\n" \
    "ds_read_b128 v[20:23], v217 offset:0\n" \
    "ds_read_b128 v[24:27], v218 offset:0\n" \
    "ds_read_b128 v[28:31], v219 offset:0\n" \
    "global_load_lds_dwordx4 %[xo2], s[40:41] offset:2048\n" \
    "s_waitcnt lgkmcnt(14)\n" \
    "v_mfma_f32_32x32x16_bf16 v[192:207], v[132:135], v[8:11], v[192:207]\n" \
    "ds_read_b64_tr_b16 v[164:165], v221 offset:12288\n" \
    "ds_read_b64_tr_b16 v[166:167], v221 offset:12800\n" \
    "global_load_lds_dwordx4 %[xo3], s[40:41] offset:3072\n" \
    "v_mfma_f32_32x32x16_bf16 v[176:191], v[136:139], v[12:15], v[176:191]\n" \
    "ds_read_b64_tr_b16 v[168:169], v220 offset:14336\n" \
    "ds_read_b64_tr_b16 v[170:171], v220 offset:14848\n" \
    "s_add_u32 m0, %[ring], 4096\n" \
    "s_nop 0\n" \
    "global_load_lds_dwordx4 %[xo0], s[42:43] offset:0\n" \
    "v_mfma_f32_32x32x16_bf16 v[192:207], v[140:143], v[12:15], v[192:207]\n" \
    "ds_read_b64_tr_b16 v[172:173], v221 offset:14336\n" \
    "ds_read_b64_tr_b16 v[174:175], v221 offset:14848\n" \
    "ds_read_b32 v211, v232 offset:44\n" \
    "global_load_lds_dwordx4 %[xo1], s[42:43] offset:1024\n" \
    "s_waitcnt lgkmcnt(0)\n" \
    "v_cmp_gt_u32 vcc, 4, v211\n" \
    "s_cbranch_vccz LMCM10_%=\n" \
    "s_mov_b32 %[cnt], 0x200000\n" \
    "LSPM10_%=:\n" \
    "ds_read_b32 v211, v232 offset:44\n" \
    "s_sub_u32 %[cnt], %[cnt], 1\n" \
    "s_cbranch_scc1 LGOM10_%=\n" \
    "s_waitcnt lgkmcnt(0)\n" \
    "v_cmp_gt_u32 vcc, 4, v211\n" \
    "s_cbranch_vccnz LSPM10_%=\n" \
    "LGOM10_%=:\n" \
    "s_waitcnt lgkmcnt(0)\n" \
    "LMCM10_%=:\n" \
    "v_mfma_f32_32x32x16_bf16 v[176:191], v[144:147], v[16:19], v[176:191]\n" \
    "ds_read_b64_tr_b16 v[112:113], v220 offset:16384\n" \
    "ds_read_b64_tr_b16 v[114:115], v220 offset:16896\n" \
    "global_load_lds_dwordx4 %[xo2], s[42:43] offset:2048\n" \
    "v_mfma_f32_32x32x16_bf16 v[192:207], v[148:151], v[16:19], v[192:207]\n" \
    "ds_read_b64_tr_b16 v[116:117], v221 offset:16384\n" \
    "ds_read_b64_tr_b16 v[118:119], v221 offset:16896\n" \
    "global_load_lds_dwordx4 %[xo3], s[42:43] offset:3072\n" \
    "v_mfma_f32_32x32x16_bf16 v[176:191], v[152:155], v[20:23], v[176:191]\n" \
    "ds_read_b64_tr_b16 v[120:121], v220 offset:18432\n" \
    "ds_read_b64_tr_b16 v[122:123], v220 offset:18944\n" \
    "s_add_u32 m0, %[wdst], 40960\n" \
    "s_nop 0\n" \
    "global_load_lds_dwordx4 %[vo0], s[58:59] offset:0\n" \
    "v_mfma_f32_32x32x16_bf16 v[192:207], v[156:159], v[20:23], v[192:207]\n" \
    "ds_read_b64_tr_b16 v[124:125], v221 offset:18432\n" \
    "ds_read_b64_tr_b16 v[126:127], v221 offset:18944\n" \
    "v_mfma_f32_32x32x16_bf16 v[176:191], v[160:163], v[24:27], v[176:191]\n" \
    "ds_read_b64_tr_b16 v[128:129], v220 offset:20480\n" \
    "ds_read_b64_tr_b16 v[130:131], v220 offset:20992\n" \
    "s_waitcnt vmcnt(13)\n" \
    "ds_read_b128 v[32:35], v212 offset:8192\n" \
    "ds_read_b128 v[36:39], v213 offset:8192\n" \
    "ds_read_b128 v[40:43], v214 offset:8192\n" \
    "ds_read_b128 v[44:47], v215 offset:8192\n" \
    "global_load_lds_dwordx4 %[vo1], s[58:59] offset:1024\n" \
    "v_mfma_f32_32x32x16_bf16 v[192:207], v[164:167], v[24:27], v[192:207]\n" \
    "ds_read_b64_tr_b16 v[132:133], v221 offset:20480\n" \
    "ds_read_b64_tr_b16 v[134:135], v221 offset:20992\n" \
    "s_waitcnt vmcnt(10)\n" \
    "s_mov_b64 exec, 1\n" \
    "ds_add_u32 v232, v210 offset:48\n" \
    "s_mov_b64 exec, -1\n" \
    "s_add_u32 m0, %[wdst], 49152\n" \
    "s_nop 0\n" \
    "global_load_lds_dwordx4 %[vo0], s[60:61] offset:0\n" \
    "v_mfma_f32_32x32x16_bf16 v[176:191], v[168:171], v[28:31], v[176:191]\n" \
    "ds_read_b64_tr_b16 v[136:137], v220 offset:22528\n" \
    "ds_read_b64_tr_b16 v[138:139], v220 offset:23040\n" \
    "global_load_lds_dwordx4 %[vo1], s[60:61] offset:1024\n" \
    "v_mfma_f32_32x32x16_bf16 v[192:207], v[172:175], v[28:31], v[192:207]\n" \
    "ds_read_b64_tr_b16 v[140:141], v221 offset:22528\n" \
    "ds_read_b64_tr_b16 v[142:143], v221 offset:23040\n" \
    "ds_read_b32 v211, v232 offset:48\n" \
    "s_add_u32 m0, %[lent], 0\n" \
    "s_nop 0\n" \
    "global_load_lds_dwordx4 %[xo0], s[44:45] offset:0\n" \
    "s_waitcnt lgkmcnt(0)\n" \
    "v_cmp_gt_u32 vcc, 4, v211\n" \
    "s_cbranch_vccz LMCM20_%=\n" \
    "s_mov_b32 %[cnt], 0x200000\n" \
    "LSPM20_%=:\n" \
    "ds_read_b32 v211, v232 offset:48\n" \
    "s_sub_u32 %[cnt], %[cnt], 1\n" \
    "s_cbranch_scc1 LGOM20_%=\n" \
    "s_waitcnt lgkmcnt(0)\n" \
    "v_cmp_gt_u32 vcc, 4, v211\n" \
    "s_cbranch_vccnz LSPM20_%=\n" \
    "LGOM20_%=:\n" \
    "s_waitcnt lgkmcnt(0)\n" \
    "LMCM20_%=:\n" \
    "v_mfma_f32_32x32x16_bf16 v[176:191], v[112:115], v[32:35], v[176:191]\n" \
    "ds_read_b64_tr_b16 v[144:145], v220 offset:24576\n" \
    "ds_read_b64_tr_b16 v[146:147], v220 offset:25088\n" \
    "global_load_lds_dwordx4 %[xo1], s[44:45] offset:1024\n" \
    "v_mfma_f32_32x32x16_bf16 v[192:207], v[116:119], v[32:35], v[192:207]\n" \
    "ds_read_b64_tr_b16 v[148:149], v221 offset:24576\n" \
    "ds_read_b64_tr_b16 v[150:151], v221 offset:25088\n" \
    "global_load_lds_dwordx4 %[xo2], s[44:45] offset:2048\n" \
    "v_mfma_f32_32x32x16_bf16 v[176:191], v[120:123], v[36:39], v[176:191]\n" \
    "ds_read_b64_tr_b16 v[152:153], v220 offset:26624\n" \
    "ds_read_b64_tr_b16 v[154:155], v220 offset:27136\n" \
    "global_load_lds_dwordx4 %[xo3], s[44:45] offset:3072\n" \
    "v_mfma_f32_32x32x16_bf16 v[192:207], v[124:127], v[36:39], v[192:207]\n" \
    "ds_read_b64_tr_b16 v[156:157], v221 offset:26624\n" \
    "ds_read_b64_tr_b16 v[158:159], v221 offset:27136\n" \
    "v_mfma_f32_32x32x16_bf16 v[176:191], v[128:131], v[40:43], v[176:191]\n" \
    "ds_read_b64_tr_b16 v[160:161], v220 offset:28672\n" \
    "ds_read_b64_tr_b16 v[162:163], v220 offset:29184\n" \
    "s_waitcnt vmcnt(12)\n" \
    "ds_read_b128 v[48:51], v212 offset:0\n" \
    "ds_read_b128 v[52:55], v213 offset:0\n" \
    "ds_read_b128 v[56:59], v214 offset:0\n" \
    "ds_read_b128 v[60:63], v215 offset:0\n" \
    "s_add_u32 m0, %[ring], 8192\n" \
    "s_nop 0\n" \
    "global_load_lds_dwordx4 %[xo0], s[46:47] offset:0\n" \
    "v_mfma_f32_32x32x16_bf16 v[192:207], v[132:135], v[40:43], v[192:207]\n" \
    "ds_read_b64_tr_b16 v[164:165], v221 offset:28672\n" \
    "ds_read_b64_tr_b16 v[166:167], v221 offset:29184\n" \
    "global_load_lds_dwordx4 %[xo1], s[46:47] offset:1024\n" \
    "v_mfma_f32_32x32x16_bf16 v[176:191], v[136:139], v[44:47], v[176:191]\n" \
    "ds_read_b64_tr_b16 v[168:169], v220 offset:30720\n" \
    "ds_read_b64_tr_b16 v[170:171], v220 offset:31232\n" \
    "global_load_lds_dwordx4 %[xo2], s[46:47] offset:2048\n" \
    "v_mfma_f32_32x32x16_bf16 v[192:207], v[140:143], v[44:47], v[192:207]\n" \
    "ds_read_b64_tr_b16 v[172:173], v221 offset:30720\n" \
    "ds_read_b64_tr_b16 v[174:175], v221 offset:31232\n" \
    "global_load_lds_dwordx4 %[xo3], s[46:47] offset:3072\n" \
    "s_waitcnt lgkmcnt(9)\n" \
    "v_mfma_f32_32x32x16_bf16 v[176:191], v[144:147], v[48:51], v[176:191]\n" \
    "ds_read_b64_tr_b16 v[112:113], v220 offset:32768\n" \
    "ds_read_b64_tr_b16 v[114:115], v220 offset:33280\n" \
    "global_load_dwordx2 v[208:209], v224, s[62:63] sc1\n" \
    "v_mfma_f32_32x32x16_bf16 v[192:207], v[148:151], v[48:51], v[192:207]\n" \
    "ds_read_b64_tr_b16 v[116:117], v221 offset:32768\n" \
    "ds_read_b64_tr_b16 v[118:119], v221 offset:33280\n" \
    "s_waitcnt lgkmcnt(10)\n" \
    "s_add_u32 m0, %[ring], 0\n" \
    "s_nop 0\n" \
    "global_load_lds_dwordx4 v225, s[48:49]\n" \
    "v_mfma_f32_32x32x16_bf16 v[176:191], v[152:155], v[52:55], v[176:191]\n" \
    "ds_read_b64_tr_b16 v[120:121], v220 offset:34816\n" \
    "ds_read_b64_tr_b16 v[122:123], v220 offset:35328\n" \
    "s_add_u32 m0, %[ring], 1024\n" \
    "s_nop 0\n" \
    "global_load_lds_dwordx4 v225, s[50:51]\n" \
    "v_mfma_f32_32x32x16_bf16 v[192:207], v[156:159], v[52:55], v[192:207]\n" \
    "ds_read_b64_tr_b16 v[124:125], v221 offset:34816\n" \
    "ds_read_b64_tr_b16 v[126:127], v221 offset:35328\n" \
    "s_waitcnt vmcnt(11)\n" \
    "s_mov_b64 exec, 1\n" \
    "ds_add_u32 v232, v210 offset:52\n" \
    "s_mov_b64 exec, -1\n" \
    "v_mfma_f32_32x32x16_bf16 v[176:191], v[160:163], v[56:59], v[176:191]\n" \
    "ds_read_b64_tr_b16 v[128:129], v220 offset:36864\n" \
    "ds_read_b64_tr_b16 v[130:131], v220 offset:37376\n" \
    "ds_read_b128 v[64:67], v212 offset:4096\n" \
    "ds_read_b128 v[68:71], v213 offset:4096\n" \
    "ds_read_b128 v[72:75], v214 offset:4096\n" \
    "ds_read_b128 v[76:79], v215 offset:4096\n" \
    "s_add_u32 m0, %[ring], 2048\n" \
    "s_nop 0\n" \
    "global_load_lds_dwordx4 v225, s[52:53]\n" \
    "s_waitcnt lgkmcnt(14)\n" \
    "v_mfma_f32_32x32x16_bf16 v[192:207], v[164:167], v[56:59], v[192:207]\n" \
    "ds_read_b64_tr_b16 v[132:133], v221 offset:36864\n" \
    "ds_read_b64_tr_b16 v[134:135], v221 offset:37376\n" \
    "s_add_u32 m0, %[ring], 3072\n" \
    "s_nop 0\n" \
    "global_load_lds_dwordx4 v225, s[54:55]\n" \
    "v_mfma_f32_32x32x16_bf16 v[176:191], v[168:171], v[60:63], v[176:191]\n" \
    "ds_read_b64_tr_b16 v[136:137], v220 offset:38912\n" \
    "ds_read_b64_tr_b16 v[138:139], v220 offset:39424\n" \
    "s_waitcnt lgkmcnt(4)\n" \
    "s_add_u32 m0, %[ring], 4096\n" \
    "s_nop 0\n" \
    "global_load_lds_dwordx4 v225, s[56:57]\n" \
    "v_mfma_f32_32x32x16_bf16 v[192:207], v[172:175], v[60:63], v[192:207]\n" \
    "ds_read_b64_tr_b16 v[140:141], v221 offset:38912\n" \
    "ds_read_b64_tr_b16 v[142:143], v221 offset:39424\n" \
    "ds_read_b32 v211, v232 offset:52\n" \
    "s_add_u32 m0, %[ring], 5120\n" \
    "s_nop 0\n" \
    "global_load_lds_dwordx4 v225, s[58:59]\n" \
    "s_waitcnt lgkmcnt(0)\n" \
    "v_cmp_gt_u32 vcc, 4, v211\n" \
    "s_cbranch_vccz LMCM40_%=\n" \
    "s_mov_b32 %[cnt], 0x200000\n" \
    "LSPM40_%=:\n" \
    "ds_read_b32 v211, v232 offset:52\n" \
    "s_sub_u32 %[cnt], %[cnt], 1\n" \
    "s_cbranch_scc1 LGOM40_%=\n" \
    "s_waitcnt lgkmcnt(0)\n" \
    "v_cmp_gt_u32 vcc, 4, v211\n" \
    "s_cbranch_vccnz LSPM40_%=\n" \
    "LGOM40_%=:\n" \
    "s_waitcnt lgkmcnt(0)\n" \
    "LMCM40_%=:\n" \
    "v_mfma_f32_32x32x16_bf16 v[176:191], v[112:115], v[64:67], v[176:191]\n" \
    "ds_read_b64_tr_b16 v[144:145], v220 offset:40960\n" \
    "ds_read_b64_tr_b16 v[146:147], v220 offset:41472\n" \
    "s_add_u32 m0, %[ring], 6144\n" \
    "s_nop 0\n" \
    "global_load_lds_dwordx4 v225, s[60:61]\n" \
    "v_mfma_f32_32x32x16_bf16 v[192:207], v[116:119], v[64:67], v[192:207]\n" \
    "ds_read_b64_tr_b16 v[148:149], v221 offset:40960\n" \
    "ds_read_b64_tr_b16 v[150:151], v221 offset:41472\n" \
    "v_mfma_f32_32x32x16_bf16 v[176:191], v[120:123], v[68:71], v[176:191]\n" \
    "ds_read_b64_tr_b16 v[152:153], v220 offset:43008\n" \
    "ds_read_b64_tr_b16 v[154:155], v220 offset:43520\n" \
    "v_mfma_f32_32x32x16_bf16 v[192:207], v[124:127], v[68:71], v[192:207]\n" \
    "ds_read_b64_tr_b16 v[156:157], v221 offset:43008\n" \
    "ds_read_b64_tr_b16 v[158:159], v221 offset:43520\n" \
    "v_mfma_f32_32x32x16_bf16 v[176:191], v[128:131], v[72:75], v[176:191]\n" \
    "ds_read_b64_tr_b16 v[160:161], v220 offset:45056\n" \
    "ds_read_b64_tr_b16 v[162:163], v220 offset:45568\n" \
    "s_waitcnt vmcnt(12)\n" \
    "ds_read_b128 v[80:83], v216 offset:0\n" \
    "ds_read_b128 v[84:87], v217 offset:0\n" \
    "ds_read_b128 v[88:91], v218 offset:0\n" \
    "ds_read_b128 v[92:95], v219 offset:0\n" \
    "v_mfma_f32_32x32x16_bf16 v[192:207], v[132:135], v[72:75], v[192:207]\n" \
    "ds_read_b64_tr_b16 v[164:165], v221 offset:45056\n" \
    "ds_read_b64_tr_b16 v[166:167], v221 offset:45568\n" \
    "v_mfma_f32_32x32x16_bf16 v[176:191], v[136:139], v[76:79], v[176:191]\n" \
    "ds_read_b64_tr_b16 v[168:169], v220 offset:47104\n" \
    "ds_read_b64_tr_b16 v[170:171], v220 offset:47616\n" \
    "v_mfma_f32_32x32x16_bf16 v[192:207], v[140:143], v[76:79], v[192:207]\n" \
    "ds_read_b64_tr_b16 v[172:173], v221 offset:47104\n" \
    "ds_read_b64_tr_b16 v[174:175], v221 offset:47616\n" \
    "s_waitcnt lgkmcnt(9)\n" \
    "v_mfma_f32_32x32x16_bf16 v[176:191], v[144:147], v[80:83], v[176:191]\n" \
    "ds_read_b64_tr_b16 v[112:113], v220 offset:49152\n" \
    "ds_read_b64_tr_b16 v[114:115], v220 offset:49664\n" \
    "v_mfma_f32_32x32x16_bf16 v[192:207], v[148:151], v[80:83], v[192:207]\n" \
    "ds_read_b64_tr_b16 v[116:117], v221 offset:49152\n" \
    "ds_read_b64_tr_b16 v[118:119], v221 offset:49664\n" \
    "s_waitcnt lgkmcnt(12)\n" \
    "v_mfma_f32_32x32x16_bf16 v[176:191], v[152:155], v[84:87], v[176:191]\n" \
    "ds_read_b64_tr_b16 v[120:121], v220 offset:51200\n" \
    "ds_read_b64_tr_b16 v[122:123], v220 offset:51712\n" \
    "v_mfma_f32_32x32x16_bf16 v[192:207], v[156:159], v[84:87], v[192:207]\n" \
    "ds_read_b64_tr_b16 v[124:125], v221 offset:51200\n" \
    "ds_read_b64_tr_b16 v[126:127], v221 offset:51712\n" \
    "s_waitcnt lgkmcnt(14)\n" \
    "v_mfma_f32_32x32x16_bf16 v[176:191], v[160:163], v[88:91], v[176:191]\n" \
    "ds_read_b64_tr_b16 v[128:129], v220 offset:53248\n" \
    "ds_read_b64_tr_b16 v[130:131], v220 offset:53760\n" \
    "s_waitcnt vmcnt(8)\n" \
    "ds_read_b128 v[96:99], v212 offset:8192\n" \
    "ds_read_b128 v[100:103], v213 offset:8192\n" \
    "ds_read_b128 v[104:107], v214 offset:8192\n" \
    "ds_read_b128 v[108:111], v215 offset:8192\n" \
    "s_waitcnt lgkmcnt(14)\n" \
    "v_mfma_f32_32x32x16_bf16 v[192:207], v[164:167], v[88:91], v[192:207]\n" \
    "ds_read_b64_tr_b16 v[132:133], v221 offset:53248\n" \
    "ds_read_b64_tr_b16 v[134:135], v221 offset:53760\n" \
    "v_mfma_f32_32x32x16_bf16 v[176:191], v[168:171], v[92:95], v[176:191]\n" \
    "ds_read_b64_tr_b16 v[136:137], v220 offset:55296\n" \
    "ds_read_b64_tr_b16 v[138:139], v220 offset:55808\n" \
    "v_mfma_f32_32x32x16_bf16 v[192:207], v[172:175], v[92:95], v[192:207]\n" \
    "ds_read_b64_tr_b16 v[140:141], v221 offset:55296\n" \
    "ds_read_b64_tr_b16 v[142:143], v221 offset:55808\n" \
    "s_waitcnt lgkmcnt(9)\n" \
    "v_mfma_f32_32x32x16_bf16 v[176:191], v[112:115], v[96:99], v[176:191]\n" \
    "s_waitcnt vmcnt(7)\n" \
    "s_mov_b32 %[cnt], 0x20000\n" \
    "LGPM0_%=:\n" \
    "v_cmp_ne_u64 vcc, v[208:209], %[tag]\n" \
    "s_cbranch_vccz LGDM0_%=\n" \
    "s_sub_u32 %[cnt], %[cnt], 1\n" \
    "s_cbranch_scc1 LGDM0_%=\n" \
    "s_sleep 8\n" \
    "LGLM0_%=:\n" \
    "global_load_dwordx2 v[208:209], v224, s[62:63] sc1\n" \
    "s_waitcnt vmcnt(0)\n" \
    "s_branch LGPM0_%=\n" \
    "LGDM0_%=:\n" \
    "s_waitcnt lgkmcnt(8)\n" \
    "v_mfma_f32_32x32x16_bf16 v[176:191], v[120:123], v[100:103], v[176:191]\n" \
    "s_waitcnt lgkmcnt(7)\n" \
    "v_mfma_f32_32x32x16_bf16 v[176:191], v[128:131], v[104:107], v[176:191]\n" \
    "s_waitcnt lgkmcnt(2)\n" \
    "v_mfma_f32_32x32x16_bf16 v[176:191], v[136:139], v[108:111], v[176:191]\n" \
    "v_mfma_f32_32x32x16_bf16 v[192:207], v[116:119], v[96:99], v[192:207]\n" \
    "s_mov_b32 s40, s62\n" \
    "s_and_b32 s41, s63, 0xffff\n" \
    "s_mov_b32 s42, %[wend]\n" \
    "s_mov_b32 s43, 0x20000\n" \
    "s_movk_i32 s44, 4096\n" \
    "v_mfma_f32_32x32x16_bf16 v[192:207], v[124:127], v[100:103], v[192:207]\n" \
    "s_mov_b32 s48, 0xfffffff\n" \
    "s_mov_b32 s49, 0xfffffff\n" \
    "s_mov_b32 s46, 0x3e8b97f6\n" \
    "s_mov_b32 s47, 0x3e8b97f6\n" \
    "s_mov_b32 m0, %[m0s]\n" \
    "v_mfma_f32_32x32x16_bf16 v[192:207], v[132:135], v[104:107], v[192:207]\n" \
    "s_waitcnt lgkmcnt(0)\n" \
    "v_mfma_f32_32x32x16_bf16 v[192:207], v[140:143], v[108:111], v[192:207]\n" \
    "v_cvt_pk_bf16_f32 v144, v176, v177\n" \
    "v_cvt_pk_bf16_f32 v145, v178, v179\n" \
    "v_cvt_pk_bf16_f32 v146, v180, v181\n" \
    "v_cvt_pk_bf16_f32 v147, v182, v183\n" \
    "v_cvt_pk_bf16_f32 v148, v184, v185\n" \
    "v_cvt_pk_bf16_f32 v149, v186, v187\n" \
    "v_cvt_pk_bf16_f32 v150, v188, v189\n" \
    "v_cvt_pk_bf16_f32 v151, v190, v191\n" \
    "global_store_dwordx4 v222, v[144:147], s[62:63] sc1\n" \
    "global_store_dwordx4 v222, v[148:151], s[34:35] sc1\n" \
    "s_mov_b32 vcc_lo, 0x10000000\n" \
    "s_mov_b32 vcc_hi, 0x10000000\n" \
    "v_mov_b32 v211, 0x3f803f80\n" \
    "s_nop 0\n" \
    "v_cndmask_b32 v168, 0, v211, vcc\n" \
    "v_cndmask_b32 v169, 0, v211, vcc\n" \
    "v_cvt_pk_bf16_f32 v152, v192, v193\n" \
    "v_cvt_pk_bf16_f32 v153, v194, v195\n" \
    "v_cvt_pk_bf16_f32 v154, v196, v197\n" \
    "v_cvt_pk_bf16_f32 v155, v198, v199\n" \
    "v_cvt_pk_bf16_f32 v156, v200, v201\n" \
    "v_cvt_pk_bf16_f32 v157, v202, v203\n" \
    "v_cvt_pk_bf16_f32 v158, v204, v205\n" \
    "v_cvt_pk_bf16_f32 v159, v206, v207\n" \
    "global_store_dwordx4 v223, v[152:155], s[62:63] sc1\n" \
    "global_store_dwordx4 v223, v[156:159], s[34:35] sc1\n" \
    "s_waitcnt vmcnt(4)\n" \
    "s_waitcnt lgkmcnt(0)\n" \
    "s_branch LQ_%=\n" \
    "LM2_%=:\n" \
    "s_waitcnt vmcnt(15)\n" \
    "ds_read_b128 v[0:3], v212 offset:4096\n" \
    "ds_read_b128 v[4:7], v213 offset:4096\n" \
    "ds_read_b128 v[8:11], v214 offset:4096\n" \
    "ds_read_b128 v[12:15], v215 offset:4096\n" \
    "s_waitcnt vmcnt(12)\n" \
    "v_lshl_add_u64 %[tag], %[tag], 0, %[tc]\n" \
    "s_waitcnt lgkmcnt(0)\n" \
    "v_mov_b32 v210, 1\n" \
    "s_add_u32 m0, %[ring], 0\n" \
    "s_nop 0\n" \
    "global_load_lds_dwordx4 %[xo0], s[40:41] offset:0\n" \
    "global_load_lds_dwordx4 %[xo1], s[40:41] offset:1024\n" \
    "global_load_lds_dwordx4 %[xo2], s[40:41] offset:2048\n" \
    "global_load_lds_dwordx4 %[xo3], s[40:41] offset:3072\n" \
    "s_waitcnt vmcnt(10)\n" \
    "ds_read_b128 v[16:19], v216 offset:0\n" \
    "ds_read_b128 v[20:23], v217 offset:0\n" \
    "ds_read_b128 v[24:27], v218 offset:0\n" \
    "ds_read_b128 v[28:31], v219 offset:0\n" \
    "s_add_u32 m0, %[ring], 4096\n" \
    "s_nop 0\n" \
    "global_load_lds_dwordx4 %[xo0], s[42:43] offset:0\n" \
    "global_load_lds_dwordx4 %[xo1], s[42:43] offset:1024\n" \
    "global_load_lds_dwordx4 %[xo2], s[42:43] offset:2048\n" \
    "global_load_lds_dwordx4 %[xo3], s[42:43] offset:3072\n" \
    "s_waitcnt lgkmcnt(0)\n" \
    "s_add_u32 m0, %[lent], 0\n" \
    "s_nop 0\n" \
    "global_load_lds_dwordx4 %[xo0], s[44:45] offset:0\n" \
    "global_load_lds_dwordx4 %[xo1], s[44:45] offset:1024\n" \
    "global_load_lds_dwordx4 %[xo2], s[44:45] offset:2048\n" \
    "s_waitcnt vmcnt(11)\n" \
    "ds_read_b128 v[32:35], v212 offset:8192\n" \
    "ds_read_b128 v[36:39], v213 offset:8192\n" \
    "ds_read_b128 v[40:43], v214 offset:8192\n" \
    "ds_read_b128 v[44:47], v215 offset:8192\n" \
    "global_load_lds_dwordx4 %[xo3], s[44:45] offset:3072\n" \
    "s_waitcnt lgkmcnt(0)\n" \
    "s_add_u32 m0, %[ring], 8192\n" \
    "s_nop 0\n" \
    "global_load_lds_dwordx4 %[xo0], s[46:47] offset:0\n" \
    "global_load_lds_dwordx4 %[xo1], s[46:47] offset:1024\n" \
    "global_load_lds_dwordx4 %[xo2], s[46:47] offset:2048\n" \
    "global_load_lds_dwordx4 %[xo3], s[46:47] offset:3072\n" \
    "s_waitcnt vmcnt(12)\n" \
    "ds_read_b128 v[48:51], v212 offset:0\n" \
    "ds_read_b128 v[52:55], v213 offset:0\n" \
    "ds_read_b128 v[56:59], v214 offset:0\n" \
    "ds_read_b128 v[60:63], v215 offset:0\n" \
    "s_waitcnt lgkmcnt(0)\n" \
    "s_add_u32 m0, %[ring], 0\n" \
    "s_nop 0\n" \
    "global_load_lds_dwordx4 v225, s[48:49]\n" \
    "s_add_u32 m0, %[ring], 1024\n" \
    "s_nop 0\n" \
    "global_load_lds_dwordx4 v225, s[50:51]\n" \
    "s_add_u32 m0, %[ring], 2048\n" \
    "s_nop 0\n" \
    "global_load_lds_dwordx4 v225, s[52:53]\n" \
    "s_add_u32 m0, %[ring], 3072\n" \
    "s_nop 0\n" \
    "global_load_lds_dwordx4 v225, s[54:55]\n" \
    "s_waitcnt vmcnt(12)\n" \
    "ds_read_b128 v[64:67], v212 offset:4096\n" \
    "ds_read_b128 v[68:71], v213 offset:4096\n" \
    "ds_read_b128 v[72:75], v214 offset:4096\n" \
    "ds_read_b128 v[76:79], v215 offset:4096\n" \
    "s_waitcnt lgkmcnt(0)\n" \
    "s_add_u32 m0, %[ring], 4096\n" \
    "s_nop 0\n" \
    "global_load_lds_dwordx4 v225, s[56:57]\n" \
    "s_add_u32 m0, %[ring], 5120\n" \
    "s_nop 0\n" \
    "global_load_lds_dwordx4 v225, s[58:59]\n" \
    "s_add_u32 m0, %[ring], 6144\n" \
    "s_nop 0\n" \
    "global_load_lds_dwordx4 v225, s[60:61]\n" \
    "global_load_dwordx2 v[208:209], v224, s[62:63] sc1\n" \
    "s_waitcnt vmcnt(12)\n" \
    "ds_read_b128 v[80:83], v216 offset:0\n" \
    "ds_read_b128 v[84:87], v217 offset:0\n" \
    "ds_read_b128 v[88:91], v218 offset:0\n" \
    "ds_read_b128 v[92:95], v219 offset:0\n" \
    "s_waitcnt vmcnt(8)\n" \
    "ds_read_b128 v[96:99], v212 offset:8192\n" \
    "ds_read_b128 v[100:103], v213 offset:8192\n" \
    "ds_read_b128 v[104:107], v214 offset:8192\n" \
    "ds_read_b128 v[108:111], v215 offset:8192\n" \
    "s_waitcnt vmcnt(0)\n" \
    "s_mov_b32 %[cnt], 0x20000\n" \
    "LGPM2_%=:\n" \
    "v_cmp_ne_u64 vcc, v[208:209], %[tag]\n" \
    "s_cbranch_vccz LGDM2_%=\n" \
    "s_sub_u32 %[cnt], %[cnt], 1\n" \
    "s_cbranch_scc1 LGDM2_%=\n" \
    "s_sleep 8\n" \
    "LGLM2_%=:\n" \
    "global_load_dwordx2 v[208:209], v224, s[62:63] sc1\n" \
    "s_waitcnt vmcnt(0)\n" \
    "s_branch LGPM2_%=\n" \
    "LGDM2_%=:\n" \
    "s_mov_b32 s40, s62\n" \
    "s_and_b32 s41, s63, 0xffff\n" \
    "s_mov_b32 s42, %[wend]\n" \
    "s_mov_b32 s43, 0x20000\n" \
    "s_movk_i32 s44, 4096\n" \
    "s_mov_b32 s48, 0xfffffff\n" \
    "s_mov_b32 s49, 0xfffffff\n" \
    "s_mov_b32 s46, 0x3e8b97f6\n" \
    "s_mov_b32 s47, 0x3e8b97f6\n" \
    "s_mov_b32 m0, %[m0s]\n" \
    "s_mov_b32 vcc_lo, 0x10000000\n" \
    "s_mov_b32 vcc_hi, 0x10000000\n" \
    "v_mov_b32 v211, 0x3f803f80\n" \
    "s_nop 0\n" \
    "v_cndmask_b32 v168, 0, v211, vcc\n" \
    "v_cndmask_b32 v169, 0, v211, vcc\n" \
    "s_waitcnt lgkmcnt(0)\n" \
    "s_branch LQ_%=\n" \
    "LM1_%=:\n" \
    "s_waitcnt vmcnt(15)\n" \
    "ds_read_b128 v[0:3], v212 offset:4096\n" \
    "ds_read_b128 v[4:7], v213 offset:4096\n" \
    "ds_read_b128 v[8:11], v214 offset:4096\n" \
    "ds_read_b128 v[12:15], v215 offset:4096\n" \
    "s_waitcnt vmcnt(12)\n" \
    "v_lshl_add_u64 %[tag], %[tag], 0, %[tc]\n" \
    "s_waitcnt vmcnt(10)\n" \
    "v_mov_b32 v113, 1\n" \
    "s_mov_b64 exec, 1\n" \
    "ds_add_u32 v232, v113 offset:16\n" \
    "s_mov_b64 exec, -1\n" \
    "s_add_u32 m0, %[wdst], 24576\n" \
    "s_nop 0\n" \
    "global_load_lds_dwordx4 %[vo0], s[54:55] offset:0\n" \
    "global_load_lds_dwordx4 %[vo1], s[54:55] offset:1024\n" \
    "s_mov_b32 %[cnt], 0x200000\n" \
    "LSPW1_%=:\n" \
    "ds_read_b32 v112, v232 offset:16\n" \
    "s_sub_u32 %[cnt], %[cnt], 1\n" \
    "s_cbranch_scc1 LGOW1_%=\n" \
    "s_waitcnt lgkmcnt(0)\n" \
    "v_cmp_gt_u32 vcc, 4, v112\n" \
    "s_cbranch_vccnz LSPW1_%=\n" \
    "LGOW1_%=:\n" \
    "s_waitcnt lgkmcnt(0)\n" \
    "ds_read_b64_tr_b16 v[112:113], v220 offset:0\n" \
    "ds_read_b64_tr_b16 v[114:115], v220 offset:512\n" \
    "ds_read_b64_tr_b16 v[116:117], v221 offset:0\n" \
    "ds_read_b64_tr_b16 v[118:119], v221 offset:512\n" \
    "ds_read_b64_tr_b16 v[120:121], v220 offset:2048\n" \
    "ds_read_b64_tr_b16 v[122:123], v220 offset:2560\n" \
    "ds_read_b64_tr_b16 v[124:125], v221 offset:2048\n" \
    "ds_read_b64_tr_b16 v[126:127], v221 offset:2560\n" \
    "ds_read_b64_tr_b16 v[128:129], v220 offset:4096\n" \
    "ds_read_b64_tr_b16 v[130:131], v220 offset:4608\n" \
    "ds_read_b64_tr_b16 v[132:133], v221 offset:4096\n" \
    "ds_read_b64_tr_b16 v[134:135], v221 offset:4608\n" \
    "ds_read_b64_tr_b16 v[136:137], v220 offset:6144\n" \
    "ds_read_b64_tr_b16 v[138:139], v220 offset:6656\n" \
    "ds_read_b64_tr_b16 v[140:141], v221 offset:6144\n" \
    "ds_read_b64_tr_b16 v[142:143], v221 offset:6656\n" \
    "v_mov_b32 v210, 1\n" \
    "s_add_u32 m0, %[wdst], 32768\n" \
    "s_nop 0\n" \
    "global_load_lds_dwordx4 %[vo0], s[56:57] offset:0\n" \
    "s_waitcnt lgkmcnt(14)\n" \
    "v_mfma_f32_32x32x16_bf16 v[176:191], v[0:3], v[112:115], 0\n" \
    "ds_read_b64_tr_b16 v[144:145], v220 offset:8192\n" \
    "ds_read_b64_tr_b16 v[146:147], v220 offset:8704\n" \
    "global_load_lds_dwordx4 %[vo1], s[56:57] offset:1024\n" \
    "s_waitcnt lgkmcnt(14)\n" \
    "v_mfma_f32_32x32x16_bf16 v[192:207], v[0:3], v[116:119], 0\n" \
    "ds_read_b64_tr_b16 v[148:149], v221 offset:8192\n" \
    "ds_read_b64_tr_b16 v[150:151], v221 offset:8704\n" \
    "s_add_u32 m0, %[ring], 0\n" \
    "s_nop 0\n" \
    "global_load_lds_dwordx4 %[xo0], s[40:41] offset:0\n" \
    "s_waitcnt lgkmcnt(14)\n" \
    "v_mfma_f32_32x32x16_bf16 v[176:191], v[4:7], v[120:123], v[176:191]\n" \
    "ds_read_b64_tr_b16 v[152:153], v220 offset:10240\n" \
    "ds_read_b64_tr_b16 v[154:155], v220 offset:10752\n" \
    "global_load_lds_dwordx4 %[xo1], s[40:41] offset:1024\n" \
    "s_waitcnt lgkmcnt(14)\n" \
    "v_mfma_f32_32x32x16_bf16 v[192:207], v[4:7], v[124:127], v[192:207]\n" \
    "ds_read_b64_tr_b16 v[156:157], v221 offset:10240\n" \
    "ds_read_b64_tr_b16 v[158:159], v221 offset:10752\n" \
    "s_waitcnt lgkmcnt(14)\n" \
    "v_mfma_f32_32x32x16_bf16 v[176:191], v[8:11], v[128:131], v[176:191]\n" \
    "ds_read_b64_tr_b16 v[160:161], v220 offset:12288\n" \
    "ds_read_b64_tr_b16 v[162:163], v220 offset:12800\n" \
    "s_waitcnt vmcnt(10)\n" \
    "s_mov_b64 exec, 1\n" \
    "ds_add_u32 v232, v210 offset:44\n" \
    "s_mov_b64 exec, -1\n" \
    "ds_read_b128 v[16:19], v216 offset:0\n" \
    "ds_read_b128 v[20:23], v217 offset:0\n" \
    "ds_read_b128 v[24:27], v218 offset:0\n" \
    "ds_read_b128 v[28:31], v219 offset:0\n" \
    "global_load_lds_dwordx4 %[xo2], s[40:41] offset:2048\n" \
    "s_waitcnt lgkmcnt(14)\n" \
    "v_mfma_f32_32x32x16_bf16 v[192:207], v[8:11], v[132:135], v[192:207]\n" \
    "ds_read_b64_tr_b16 v[164:165], v221 offset:12288\n" \
    "ds_read_b64_tr_b16 v[166:167], v221 offset:12800\n" \
    "global_load_lds_dwordx4 %[xo3], s[40:41] offset:3072\n" \
    "v_mfma_f32_32x32x16_bf16 v[176:191], v[12:15], v[136:139], v[176:191]\n" \
    "ds_read_b64_tr_b16 v[168:169], v220 offset:14336\n" \
    "ds_read_b64_tr_b16 v[170:171], v220 offset:14848\n" \
    "s_add_u32 m0, %[ring], 4096\n" \
    "s_nop 0\n" \
    "global_load_lds_dwordx4 %[xo0], s[42:43] offset:0\n" \
    "v_mfma_f32_32x32x16_bf16 v[192:207], v[12:15], v[140:143], v[192:207]\n" \
    "ds_read_b64_tr_b16 v[172:173], v221 offset:14336\n" \
    "ds_read_b64_tr_b16 v[174:175], v221 offset:14848\n" \
    "ds_read_b32 v211, v232 offset:44\n" \
    "global_load_lds_dwordx4 %[xo1], s[42:43] offset:1024\n" \
    "s_waitcnt lgkmcnt(0)\n" \
    "v_cmp_gt_u32 vcc, 4, v211\n" \
    "s_cbranch_vccz LMCM11_%=\n" \
    "s_mov_b32 %[cnt], 0x200000\n" \
    "LSPM11_%=:\n" \
    "ds_read_b32 v211, v232 offset:44\n" \
    "s_sub_u32 %[cnt], %[cnt], 1\n" \
    "s_cbranch_scc1 LGOM11_%=\n" \
    "s_waitcnt lgkmcnt(0)\n" \
    "v_cmp_gt_u32 vcc, 4, v211\n" \
    "s_cbranch_vccnz LSPM11_%=\n" \
    "LGOM11_%=:\n" \
    "s_waitcnt lgkmcnt(0)\n" \
    "LMCM11_%=:\n" \
    "v_mfma_f32_32x32x16_bf16 v[176:191], v[16:19], v[144:147], v[176:191]\n" \
    "ds_read_b64_tr_b16 v[112:113], v220 offset:16384\n" \
    "ds_read_b64_tr_b16 v[114:115], v220 offset:16896\n" \
    "global_load_lds_dwordx4 %[xo2], s[42:43] offset:2048\n" \
    "v_mfma_f32_32x32x16_bf16 v[192:207], v[16:19], v[148:151], v[192:207]\n" \
    "ds_read_b64_tr_b16 v[116:117], v221 offset:16384\n" \
    "ds_read_b64_tr_b16 v[118:119], v221 offset:16896\n" \
    "global_load_lds_dwordx4 %[xo3], s[42:43] offset:3072\n" \
    "v_mfma_f32_32x32x16_bf16 v[176:191], v[20:23], v[152:155], v[176:191]\n" \
    "ds_read_b64_tr_b16 v[120:121], v220 offset:18432\n" \
    "ds_read_b64_tr_b16 v[122:123], v220 offset:18944\n" \
    "s_add_u32 m0, %[wdst], 40960\n" \
    "s_nop 0\n" \
    "global_load_lds_dwordx4 %[vo0], s[58:59] offset:0\n" \
    "v_mfma_f32_32x32x16_bf16 v[192:207], v[20:23], v[156:159], v[192:207]\n" \
    "ds_read_b64_tr_b16 v[124:125], v221 offset:18432\n" \
    "ds_read_b64_tr_b16 v[126:127], v221 offset:18944\n" \
    "v_mfma_f32_32x32x16_bf16 v[176:191], v[24:27], v[160:163], v[176:191]\n" \
    "ds_read_b64_tr_b16 v[128:129], v220 offset:20480\n" \
    "ds_read_b64_tr_b16 v[130:131], v220 offset:20992\n" \
    "s_waitcnt vmcnt(13)\n" \
    "ds_read_b128 v[32:35], v212 offset:8192\n" \
    "ds_read_b128 v[36:39], v213 offset:8192\n" \
    "ds_read_b128 v[40:43], v214 offset:8192\n" \
    "ds_read_b128 v[44:47], v215 offset:8192\n" \
    "global_load_lds_dwordx4 %[vo1], s[58:59] offset:1024\n" \
    "v_mfma_f32_32x32x16_bf16 v[192:207], v[24:27], v[164:167], v[192:207]\n" \
    "ds_read_b64_tr_b16 v[132:133], v221 offset:20480\n" \
    "ds_read_b64_tr_b16 v[134:135], v221 offset:20992\n" \
    "s_waitcnt vmcnt(10)\n" \
    "s_mov_b64 exec, 1\n" \
    "ds_add_u32 v232, v210 offset:48\n" \
    "s_mov_b64 exec, -1\n" \
    "s_add_u32 m0, %[wdst], 49152\n" \
    "s_nop 0\n" \
    "global_load_lds_dwordx4 %[vo0], s[60:61] offset:0\n" \
    "v_mfma_f32_32x32x16_bf16 v[176:191], v[28:31], v[168:171], v[176:191]\n" \
    "ds_read_b64_tr_b16 v[136:137], v220 offset:22528\n" \
    "ds_read_b64_tr_b16 v[138:139], v220 offset:23040\n" \
    "global_load_lds_dwordx4 %[vo1], s[60:61] offset:1024\n" \
    "v_mfma_f32_32x32x16_bf16 v[192:207], v[28:31], v[172:175], v[192:207]\n" \
    "ds_read_b64_tr_b16 v[140:141], v221 offset:22528\n" \
    "ds_read_b64_tr_b16 v[142:143], v221 offset:23040\n" \
    "ds_read_b32 v211, v232 offset:48\n" \
    "s_add_u32 m0, %[lent], 0\n" \
    "s_nop 0\n" \
    "global_load_lds_dwordx4 %[xo0], s[44:45] offset:0\n" \
    "s_waitcnt lgkmcnt(0)\n" \
    "v_cmp_gt_u32 vcc, 4, v211\n" \
    "s_cbranch_vccz LMCM21_%=\n" \
    "s_mov_b32 %[cnt], 0x200000\n" \
    "LSPM21_%=:\n" \
    "ds_read_b32 v211, v232 offset:48\n" \
    "s_sub_u32 %[cnt], %[cnt], 1\n" \
    "s_cbranch_scc1 LGOM21_%=\n" \
    "s_waitcnt lgkmcnt(0)\n" \
    "v_cmp_gt_u32 vcc, 4, v211\n" \
    "s_cbranch_vccnz LSPM21_%=\n" \
    "LGOM21_%=:\n" \
    "s_waitcnt lgkmcnt(0)\n" \
    "LMCM21_%=:\n" \
    "v_mfma_f32_32x32x16_bf16 v[176:191], v[32:35], v[112:115], v[176:191]\n" \
    "ds_read_b64_tr_b16 v[144:145], v220 offset:24576\n" \
    "ds_read_b64_tr_b16 v[146:147], v220 offset:25088\n" \
    "global_load_lds_dwordx4 %[xo1], s[44:45] offset:1024\n" \
    "v_mfma_f32_32x32x16_bf16 v[192:207], v[32:35], v[116:119], v[192:207]\n" \
    "ds_read_b64_tr_b16 v[148:149], v221 offset:24576\n" \
    "ds_read_b64_tr_b16 v[150:151], v221 offset:25088\n" \
    "global_load_lds_dwordx4 %[xo2], s[44:45] offset:2048\n" \
    "v_mfma_f32_32x32x16_bf16 v[176:191], v[36:39], v[120:123], v[176:191]\n" \
    "ds_read_b64_tr_b16 v[152:153], v220 offset:26624\n" \
    "ds_read_b64_tr_b16 v[154:155], v220 offset:27136\n" \
    "global_load_lds_dwordx4 %[xo3], s[44:45] offset:3072\n" \
    "v_mfma_f32_32x32x16_bf16 v[192:207], v[36:39], v[124:127], v[192:207]\n" \
    "ds_read_b64_tr_b16 v[156:157], v221 offset:26624\n" \
    "ds_read_b64_tr_b16 v[158:159], v221 offset:27136\n" \
    "v_mfma_f32_32x32x16_bf16 v[176:191], v[40:43], v[128:131], v[176:191]\n" \
    "ds_read_b64_tr_b16 v[160:161], v220 offset:28672\n" \
    "ds_read_b64_tr_b16 v[162:163], v220 offset:29184\n" \
    "s_waitcnt vmcnt(12)\n" \
    "ds_read_b128 v[48:51], v212 offset:0\n" \
    "ds_read_b128 v[52:55], v213 offset:0\n" \
    "ds_read_b128 v[56:59], v214 offset:0\n" \
    "ds_read_b128 v[60:63], v215 offset:0\n" \
    "s_add_u32 m0, %[ring], 8192\n" \
    "s_nop 0\n" \
    "global_load_lds_dwordx4 %[xo0], s[46:47] offset:0\n" \
    "v_mfma_f32_32x32x16_bf16 v[192:207], v[40:43], v[132:135], v[192:207]\n" \
    "ds_read_b64_tr_b16 v[164:165], v221 offset:28672\n" \
    "ds_read_b64_tr_b16 v[166:167], v221 offset:29184\n" \
    "global_load_lds_dwordx4 %[xo1], s[46:47] offset:1024\n" \
    "v_mfma_f32_32x32x16_bf16 v[176:191], v[44:47], v[136:139], v[176:191]\n" \
    "ds_read_b64_tr_b16 v[168:169], v220 offset:30720\n" \
    "ds_read_b64_tr_b16 v[170:171], v220 offset:31232\n" \
    "global_load_lds_dwordx4 %[xo2], s[46:47] offset:2048\n" \
    "v_mfma_f32_32x32x16_bf16 v[192:207], v[44:47], v[140:143], v[192:207]\n" \
    "ds_read_b64_tr_b16 v[172:173], v221 offset:30720\n" \
    "ds_read_b64_tr_b16 v[174:175], v221 offset:31232\n" \
    "global_load_lds_dwordx4 %[xo3], s[46:47] offset:3072\n" \
    "s_waitcnt lgkmcnt(9)\n" \
    "v_mfma_f32_32x32x16_bf16 v[176:191], v[48:51], v[144:147], v[176:191]\n" \
    "ds_read_b64_tr_b16 v[112:113], v220 offset:32768\n" \
    "ds_read_b64_tr_b16 v[114:115], v220 offset:33280\n" \
    "global_load_dwordx2 v[208:209], v224, s[62:63] sc1\n" \
    "v_mfma_f32_32x32x16_bf16 v[192:207], v[48:51], v[148:151], v[192:207]\n" \
    "ds_read_b64_tr_b16 v[116:117], v221 offset:32768\n" \
    "ds_read_b64_tr_b16 v[118:119], v221 offset:33280\n" \
    "s_waitcnt lgkmcnt(10)\n" \
    "s_add_u32 m0, %[ring], 0\n" \
    "s_nop 0\n" \
    "global_load_lds_dwordx4 v225, s[48:49]\n" \
    "v_mfma_f32_32x32x16_bf16 v[176:191], v[52:55], v[152:155], v[176:191]\n" \
    "ds_read_b64_tr_b16 v[120:121], v220 offset:34816\n" \
    "ds_read_b64_tr_b16 v[122:123], v220 offset:35328\n" \
    "s_add_u32 m0, %[ring], 1024\n" \
    "s_nop 0\n" \
    "global_load_lds_dwordx4 v225, s[50:51]\n" \
    "v_mfma_f32_32x32x16_bf16 v[192:207], v[52:55], v[156:159], v[192:207]\n" \
    "ds_read_b64_tr_b16 v[124:125], v221 offset:34816\n" \
    "ds_read_b64_tr_b16 v[126:127], v221 offset:35328\n" \
    "s_waitcnt vmcnt(11)\n" \
    "s_mov_b64 exec, 1\n" \
    "ds_add_u32 v232, v210 offset:52\n" \
    "s_mov_b64 exec, -1\n" \
    "v_mfma_f32_32x32x16_bf16 v[176:191], v[56:59], v[160:163], v[176:191]\n" \
    "ds_read_b64_tr_b16 v[128:129], v220 offset:36864\n" \
    "ds_read_b64_tr_b16 v[130:131], v220 offset:37376\n" \
    "ds_read_b128 v[64:67], v212 offset:4096\n" \
    "ds_read_b128 v[68:71], v213 offset:4096\n" \
    "ds_read_b128 v[72:75], v214 offset:4096\n" \
    "ds_read_b128 v[76:79], v215 offset:4096\n" \
    "s_add_u32 m0, %[ring], 2048\n" \
    "s_nop 0\n" \
    "global_load_lds_dwordx4 v225, s[52:53]\n" \
    "s_waitcnt lgkmcnt(14)\n" \
    "v_mfma_f32_32x32x16_bf16 v[192:207], v[56:59], v[164:167], v[192:207]\n" \
    "ds_read_b64_tr_b16 v[132:133], v221 offset:36864\n" \
    "ds_read_b64_tr_b16 v[134:135], v221 offset:37376\n" \
    "s_add_u32 m0, %[ring], 3072\n" \
    "s_nop 0\n" \
    "global_load_lds_dwordx4 v225, s[54:55]\n" \
    "v_mfma_f32_32x32x16_bf16 v[176:191], v[60:63], v[168:171], v[176:191]\n" \
    "ds_read_b64_tr_b16 v[136:137], v220 offset:38912\n" \
    "ds_read_b64_tr_b16 v[138:139], v220 offset:39424\n" \
    "s_waitcnt lgkmcnt(4)\n" \
    "s_add_u32 m0, %[ring], 4096\n" \
    "s_nop 0\n" \
    "global_load_lds_dwordx4 v225, s[56:57]\n" \
    "v_mfma_f32_32x32x16_bf16 v[192:207], v[60:63], v[172:175], v[192:207]\n" \
    "ds_read_b64_tr_b16 v[140:141], v221 offset:38912\n" \
    "ds_read_b64_tr_b16 v[142:143], v221 offset:39424\n" \
    "ds_read_b32 v211, v232 offset:52\n" \
    "s_add_u32 m0, %[ring], 5120\n" \
    "s_nop 0\n" \
    "global_load_lds_dwordx4 v225, s[58:59]\n" \
    "s_waitcnt lgkmcnt(0)\n" \
    "v_cmp_gt_u32 vcc, 4, v211\n" \
    "s_cbranch_vccz LMCM41_%=\n" \
    "s_mov_b32 %[cnt], 0x200000\n" \
    "LSPM41_%=:\n" \
    "ds_read_b32 v211, v232 offset:52\n" \
    "s_sub_u32 %[cnt], %[cnt], 1\n" \
    "s_cbranch_scc1 LGOM41_%=\n" \
    "s_waitcnt lgkmcnt(0)\n" \
    "v_cmp_gt_u32 vcc, 4, v211\n" \
    "s_cbranch_vccnz LSPM41_%=\n" \
    "LGOM41_%=:\n" \
    "s_waitcnt lgkmcnt(0)\n" \
    "LMCM41_%=:\n" \
    "v_mfma_f32_32x32x16_bf16 v[176:191], v[64:67], v[112:115], v[176:191]\n" \
    "ds_read_b64_tr_b16 v[144:145], v220 offset:40960\n" \
    "ds_read_b64_tr_b16 v[146:147], v220 offset:41472\n" \
    "s_add_u32 m0, %[ring], 6144\n" \
    "s_nop 0\n" \
    "global_load_lds_dwordx4 v225, s[60:61]\n" \
    "v_mfma_f32_32x32x16_bf16 v[192:207], v[64:67], v[116:119], v[192:207]\n" \
    "ds_read_b64_tr_b16 v[148:149], v221 offset:40960\n" \
    "ds_read_b64_tr_b16 v[150:151], v221 offset:41472\n" \
    "v_mfma_f32_32x32x16_bf16 v[176:191], v[68:71], v[120:123], v[176:191]\n" \
    "ds_read_b64_tr_b16 v[152:153], v220 offset:43008\n" \
    "ds_read_b64_tr_b16 v[154:155], v220 offset:43520\n" \
    "v_mfma_f32_32x32x16_bf16 v[192:207], v[68:71], v[124:127], v[192:207]\n" \
    "ds_read_b64_tr_b16 v[156:157], v221 offset:43008\n" \
    "ds_read_b64_tr_b16 v[158:159], v221 offset:43520\n" \
    "v_mfma_f32_32x32x16_bf16 v[176:191], v[72:75], v[128:131], v[176:191]\n" \
    "ds_read_b64_tr_b16 v[160:161], v220 offset:45056\n" \
    "ds_read_b64_tr_b16 v[162:163], v220 offset:45568\n" \
    "s_waitcnt vmcnt(12)\n" \
    "ds_read_b128 v[80:83], v216 offset:0\n" \
    "ds_read_b128 v[84:87], v217 offset:0\n" \
    "ds_read_b128 v[88:91], v218 offset:0\n" \
    "ds_read_b128 v[92:95], v219 offset:0\n" \
    "v_mfma_f32_32x32x16_bf16 v[192:207], v[72:75], v[132:135], v[192:207]\n" \
    "ds_read_b64_tr_b16 v[164:165], v221 offset:45056\n" \
    "ds_read_b64_tr_b16 v[166:167], v221 offset:45568\n" \
    "v_mfma_f32_32x32x16_bf16 v[176:191], v[76:79], v[136:139], v[176:191]\n" \
    "ds_read_b64_tr_b16 v[168:169], v220 offset:47104\n" \
    "ds_read_b64_tr_b16 v[170:171], v220 offset:47616\n" \
    "v_mfma_f32_32x32x16_bf16 v[192:207], v[76:79], v[140:143], v[192:207]\n" \
    "ds_read_b64_tr_b16 v[172:173], v221 offset:47104\n" \
    "ds_read_b64_tr_b16 v[174:175], v221 offset:47616\n" \
    "s_waitcnt lgkmcnt(9)\n" \
    "v_mfma_f32_32x32x16_bf16 v[176:191], v[80:83], v[144:147], v[176:191]\n" \
    "ds_read_b64_tr_b16 v[112:113], v220 offset:49152\n" \
    "ds_read_b64_tr_b16 v[114:115], v220 offset:49664\n" \
    "v_mfma_f32_32x32x16_bf16 v[192:207], v[80:83], v[148:151], v[192:207]\n" \
    "ds_read_b64_tr_b16 v[116:117], v221 offset:49152\n" \
    "ds_read_b64_tr_b16 v[118:119], v221 offset:49664\n" \
    "s_waitcnt lgkmcnt(12)\n" \
    "v_mfma_f32_32x32x16_bf16 v[176:191], v[84:87], v[152:155], v[176:191]\n" \
    "ds_read_b64_tr_b16 v[120:121], v220 offset:51200\n" \
    "ds_read_b64_tr_b16 v[122:123], v220 offset:51712\n" \
    "v_mfma_f32_32x32x16_bf16 v[192:207], v[84:87], v[156:159], v[192:207]\n" \
    "ds_read_b64_tr_b16 v[124:125], v221 offset:51200\n" \
    "ds_read_b64_tr_b16 v[126:127], v221 offset:51712\n" \
    "s_waitcnt lgkmcnt(14)\n" \
    "v_mfma_f32_32x32x16_bf16 v[176:191], v[88:91], v[160:163], v[176:191]\n" \
    "ds_read_b64_tr_b16 v[128:129], v220 offset:53248\n" \
    "ds_read_b64_tr_b16 v[130:131], v220 offset:53760\n" \
    "s_waitcnt vmcnt(8)\n" \
    "ds_read_b128 v[96:99], v212 offset:8192\n" \
    "ds_read_b128 v[100:103], v213 offset:8192\n" \
    "ds_read_b128 v[104:107], v214 offset:8192\n" \
    "ds_read_b128 v[108:111], v215 offset:8192\n" \
    "s_waitcnt lgkmcnt(14)\n" \
    "v_mfma_f32_32x32x16_bf16 v[192:207], v[88:91], v[164:167], v[192:207]\n" \
    "ds_read_b64_tr_b16 v[132:133], v221 offset:53248\n" \
    "ds_read_b64_tr_b16 v[134:135], v221 offset:53760\n" \
    "v_mfma_f32_32x32x16_bf16 v[176:191], v[92:95], v[168:171], v[176:191]\n" \
    "ds_read_b64_tr_b16 v[136:137], v220 offset:55296\n" \
    "ds_read_b64_tr_b16 v[138:139], v220 offset:55808\n" \
    "v_mfma_f32_32x32x16_bf16 v[192:207], v[92:95], v[172:175], v[192:207]\n" \
    "ds_read_b64_tr_b16 v[140:141], v221 offset:55296\n" \
    "ds_read_b64_tr_b16 v[142:143], v221 offset:55808\n" \
    "s_waitcnt lgkmcnt(9)\n" \
    "v_mfma_f32_32x32x16_bf16 v[176:191], v[96:99], v[112:115], v[176:191]\n" \
    "s_waitcnt vmcnt(7)\n" \
    "s_mov_b32 %[cnt], 0x20000\n" \
    "LGPM1_%=:\n" \
    "v_cmp_ne_u64 vcc, v[208:209], %[tag]\n" \
    "s_cbranch_vccz LGDM1_%=\n" \
    "s_sub_u32 %[cnt], %[cnt], 1\n" \
    "s_cbranch_scc1 LGDM1_%=\n" \
    "s_sleep 8\n" \
    "LGLM1_%=:\n" \
    "global_load_dwordx2 v[208:209], v224, s[62:63] sc1\n" \
    "s_waitcnt vmcnt(0)\n" \
    "s_branch LGPM1_%=\n" \
    "LGDM1_%=:\n" \
    "s_waitcnt lgkmcnt(8)\n" \
    "v_mfma_f32_32x32x16_bf16 v[176:191], v[100:103], v[120:123], v[176:191]\n" \
    "s_waitcnt lgkmcnt(7)\n" \
    "v_mfma_f32_32x32x16_bf16 v[176:191], v[104:107], v[128:131], v[176:191]\n" \
    "s_waitcnt lgkmcnt(2)\n" \
    "v_mfma_f32_32x32x16_bf16 v[176:191], v[108:111], v[136:139], v[176:191]\n" \
    "v_mfma_f32_32x32x16_bf16 v[192:207], v[96:99], v[116:119], v[192:207]\n" \
    "s_mov_b32 s40, s62\n" \
    "s_and_b32 s41, s63, 0xffff\n" \
    "s_mov_b32 s42, %[wend]\n" \
    "s_mov_b32 s43, 0x20000\n" \
    "s_movk_i32 s44, 4096\n" \
    "v_mfma_f32_32x32x16_bf16 v[192:207], v[100:103], v[124:127], v[192:207]\n" \
    "s_mov_b32 s48, 0xfffffff\n" \
    "s_mov_b32 s49, 0xfffffff\n" \
    "s_mov_b32 s46, 0x3e8b97f6\n" \
    "s_mov_b32 s47, 0x3e8b97f6\n" \
    "s_mov_b32 m0, %[m0s]\n" \
    "v_mfma_f32_32x32x16_bf16 v[192:207], v[104:107], v[132:135], v[192:207]\n" \
    "s_waitcnt lgkmcnt(0)\n" \
    "v_mfma_f32_32x32x16_bf16 v[192:207], v[108:111], v[140:143], v[192:207]\n" \
    "v_cvt_pk_bf16_f32 v144, v176, v177\n" \
    "v_cvt_pk_bf16_f32 v145, v178, v179\n" \
    "v_cvt_pk_bf16_f32 v146, v180, v181\n" \
    "v_cvt_pk_bf16_f32 v147, v182, v183\n" \
    "v_cvt_pk_bf16_f32 v148, v184, v185\n" \
    "v_cvt_pk_bf16_f32 v149, v186, v187\n" \
    "v_cvt_pk_bf16_f32 v150, v188, v189\n" \
    "v_cvt_pk_bf16_f32 v151, v190, v191\n" \
    "global_store_dwordx4 v222, v[144:147], s[62:63] sc1\n" \
    "global_store_dwordx4 v222, v[148:151], s[34:35] sc1\n" \
    "s_mov_b32 vcc_lo, 0x10000000\n" \
    "s_mov_b32 vcc_hi, 0x10000000\n" \
    "v_mov_b32 v211, 0x3f803f80\n" \
    "s_nop 0\n" \
    "v_cndmask_b32 v168, 0, v211, vcc\n" \
    "v_cndmask_b32 v169, 0, v211, vcc\n" \
    "v_cvt_pk_bf16_f32 v152, v192, v193\n" \
    "v_cvt_pk_bf16_f32 v153, v194, v195\n" \
    "v_cvt_pk_bf16_f32 v154, v196, v197\n" \
    "v_cvt_pk_bf16_f32 v155, v198, v199\n" \
    "v_cvt_pk_bf16_f32 v156, v200, v201\n" \
    "v_cvt_pk_bf16_f32 v157, v202, v203\n" \
    "v_cvt_pk_bf16_f32 v158, v204, v205\n" \
    "v_cvt_pk_bf16_f32 v159, v206, v207\n" \
    "global_store_dwordx4 v223, v[152:155], s[62:63] sc1\n" \
    "global_store_dwordx4 v223, v[156:159], s[34:35] sc1\n" \
    "s_waitcnt vmcnt(4)\n" \
    "s_waitcnt lgkmcnt(0)\n" \
    "LQ_%=:\n" \
    "s_mov_b64 exec, 1\n" \
    "ds_add_u32 v232, v210 offset:20\n" \
    "s_cmp_eq_u32 %[hj], 6\n" \
    "s_cbranch_scc0 LNH_%=\n" \
    "v_mov_b32 v208, s80\n" \
    "global_store_dwordx2 v208, %[tag], s[62:63] sc1\n" \
    "LNH_%=:\n" \
    "s_mov_b64 exec, -1\n" \
    "v_add_u32 v206, 66560, v236\n" \
    "v_add_u32 v207, 66560, v237\n" \
    "v_mov_b32 v203, 1\n" \
    "v_mov_b32 v205, s82\n" \
    "s_mov_b32 %[cnt], 0x200000\n" \
    "LSPQ_%=:\n" \
    "ds_read_b32 v112, v232 offset:20\n" \
    "s_sub_u32 %[cnt], %[cnt], 1\n" \
    "s_cbranch_scc1 LGOQ_%=\n" \
    "s_waitcnt lgkmcnt(0)\n" \
    "v_cmp_gt_u32 vcc, 4, v112\n" \
    "s_cbranch_vccnz LSPQ_%=\n" \
    "LGOQ_%=:\n" \
    "s_waitcnt lgkmcnt(0)\n" \
    "s_cmp_eq_u32 %[mode], 2\n" \
    "ds_read_b64_tr_b16 v[112:113], v226 offset:0\n" \
    "ds_read_b64_tr_b16 v[114:115], v226 offset:256\n" \
    "ds_read_b64_tr_b16 v[116:117], v226 offset:12288\n" \
    "ds_read_b64_tr_b16 v[118:119], v226 offset:12544\n" \
    "ds_read_b64_tr_b16 v[120:121], v226 offset:24576\n" \
    "ds_read_b64_tr_b16 v[122:123], v226 offset:24832\n" \
    "ds_read_b64_tr_b16 v[124:125], v226 offset:36864\n" \
    "ds_read_b64_tr_b16 v[126:127], v226 offset:37120\n" \
    "ds_read_b64_tr_b16 v[128:129], v226 offset:1024\n" \
    "ds_read_b64_tr_b16 v[130:131], v226 offset:1280\n" \
    "ds_read_b64_tr_b16 v[132:133], v226 offset:13312\n" \
    "ds_read_b64_tr_b16 v[134:135], v226 offset:13568\n" \
    "s_waitcnt lgkmcnt(10)\n" \
    "v_mfma_f32_32x32x16_bf16 v[152:167], v[112:115], v[0:3], 0\n" \
    "v_mov_b64 v[170:171], v[168:169]\n" \
    "ds_read_b64_tr_b16 v[112:113], v226 offset:25600\n" \
    "ds_read_b64_tr_b16 v[114:115], v226 offset:25856\n" \
    "s_waitcnt lgkmcnt(10)\n" \
    "v_mfma_f32_32x32x16_bf16 v[152:167], v[116:119], v[4:7], v[152:167]\n" \
    "v_mov_b64 v[172:173], v[168:169]\n" \
    "ds_read_b64_tr_b16 v[116:117], v226 offset:37888\n" \
    "ds_read_b64_tr_b16 v[118:119], v226 offset:38144\n" \
    "s_waitcnt lgkmcnt(10)\n" \
    "v_mfma_f32_32x32x16_bf16 v[152:167], v[120:123], v[8:11], v[152:167]\n" \
    "v_mov_b64 v[174:175], v[168:169]\n" \
    "ds_read_b64_tr_b16 v[120:121], v226 offset:2048\n" \
    "ds_read_b64_tr_b16 v[122:123], v226 offset:2304\n" \
    "s_waitcnt lgkmcnt(10)\n" \
    "v_mfma_f32_32x32x16_bf16 v[152:167], v[124:127], v[12:15], v[152:167]\n" \
    "v_mov_b64 v[176:177], v[168:169]\n" \
    "ds_read_b64_tr_b16 v[124:125], v226 offset:14336\n" \
    "ds_read_b64_tr_b16 v[126:127], v226 offset:14592\n" \
    "s_waitcnt lgkmcnt(10)\n" \
    "v_mfma_f32_32x32x16_bf16 v[152:167], v[128:131], v[16:19], v[152:167]\n" \
    "v_mov_b64 v[178:179], v[168:169]\n" \
    "ds_read_b64_tr_b16 v[128:129], v226 offset:26624\n" \
    "ds_read_b64_tr_b16 v[130:131], v226 offset:26880\n" \
    "s_waitcnt lgkmcnt(10)\n" \
    "v_mfma_f32_32x32x16_bf16 v[152:167], v[132:135], v[20:23], v[152:167]\n" \
    "v_mov_b64 v[180:181], v[168:169]\n" \
    "ds_read_b64_tr_b16 v[132:133], v226 offset:38912\n" \
    "ds_read_b64_tr_b16 v[134:135], v226 offset:39168\n" \
    "s_waitcnt lgkmcnt(10)\n" \
    "v_mfma_f32_32x32x16_bf16 v[152:167], v[112:115], v[24:27], v[152:167]\n" \
    "v_mov_b64 v[182:183], v[168:169]\n" \
    "ds_read_b64_tr_b16 v[112:113], v226 offset:3072\n" \
    "ds_read_b64_tr_b16 v[114:115], v226 offset:3328\n" \
    "s_waitcnt lgkmcnt(10)\n" \
    "v_mfma_f32_32x32x16_bf16 v[152:167], v[116:119], v[28:31], v[152:167]\n" \
    "v_mov_b64 v[184:185], v[168:169]\n" \
    "ds_read_b64_tr_b16 v[116:117], v226 offset:15360\n" \
    "ds_read_b64_tr_b16 v[118:119], v226 offset:15616\n" \
    "s_waitcnt lgkmcnt(10)\n" \
    "v_mfma_f32_32x32x16_bf16 v[152:167], v[120:123], v[32:35], v[152:167]\n" \
    "v_mov_b64 v[186:187], v[168:169]\n" \
    "ds_read_b64_tr_b16 v[120:121], v226 offset:27648\n" \
    "ds_read_b64_tr_b16 v[122:123], v226 offset:27904\n" \
    "s_waitcnt lgkmcnt(10)\n" \
    "v_mfma_f32_32x32x16_bf16 v[152:167], v[124:127], v[36:39], v[152:167]\n" \
    "v_mov_b64 v[188:189], v[168:169]\n" \
    "ds_read_b64_tr_b16 v[124:125], v226 offset:39936\n" \
    "ds_read_b64_tr_b16 v[126:127], v226 offset:40192\n" \
    "s_waitcnt lgkmcnt(10)\n" \
    "v_mfma_f32_32x32x16_bf16 v[152:167], v[128:131], v[40:43], v[152:167]\n" \
    "v_mov_b64 v[190:191], v[168:169]\n" \
    "ds_read_b64_tr_b16 v[128:129], v226 offset:4096\n" \
    "ds_read_b64_tr_b16 v[130:131], v226 offset:4352\n" \
    "s_waitcnt lgkmcnt(10)\n" \
    "v_mfma_f32_32x32x16_bf16 v[152:167], v[132:135], v[44:47], v[152:167]\n" \
    "v_mov_b64 v[192:193], v[168:169]\n" \
    "ds_read_b64_tr_b16 v[132:133], v226 offset:16384\n" \
    "ds_read_b64_tr_b16 v[134:135], v226 offset:16640\n" \
    "s_waitcnt lgkmcnt(10)\n" \
    "v_mfma_f32_32x32x16_bf16 v[152:167], v[112:115], v[48:51], v[152:167]\n" \
    "v_mov_b64 v[194:195], v[168:169]\n" \
    "ds_read_b64_tr_b16 v[112:113], v226 offset:28672\n" \
    "ds_read_b64_tr_b16 v[114:115], v226 offset:28928\n" \
    "s_waitcnt lgkmcnt(10)\n" \
    "v_mfma_f32_32x32x16_bf16 v[152:167], v[116:119], v[52:55], v[152:167]\n" \
    "v_mov_b64 v[196:197], v[168:169]\n" \
    "ds_read_b64_tr_b16 v[116:117], v226 offset:40960\n" \
    "ds_read_b64_tr_b16 v[118:119], v226 offset:41216\n" \
    "s_waitcnt lgkmcnt(10)\n" \
    "v_mfma_f32_32x32x16_bf16 v[152:167], v[120:123], v[56:59], v[152:167]\n" \
    "v_mov_b64 v[198:199], v[168:169]\n" \
    "ds_read_b64_tr_b16 v[120:121], v226 offset:5120\n" \
    "ds_read_b64_tr_b16 v[122:123], v226 offset:5376\n" \
    "s_waitcnt lgkmcnt(10)\n" \
    "v_mfma_f32_32x32x16_bf16 v[152:167], v[124:127], v[60:63], v[152:167]\n" \
    "ds_read_b64_tr_b16 v[124:125], v226 offset:17408\n" \
    "ds_read_b64_tr_b16 v[126:127], v226 offset:17664\n" \
    "s_waitcnt lgkmcnt(10)\n" \
    "v_mfma_f32_32x32x16_bf16 v[152:167], v[128:131], v[64:67], v[152:167]\n" \
    "ds_read_b64_tr_b16 v[128:129], v226 offset:29696\n" \
    "ds_read_b64_tr_b16 v[130:131], v226 offset:29952\n" \
    "s_waitcnt lgkmcnt(10)\n" \
    "v_mfma_f32_32x32x16_bf16 v[152:167], v[132:135], v[68:71], v[152:167]\n" \
    "ds_read_b64_tr_b16 v[132:133], v226 offset:41984\n" \
    "ds_read_b64_tr_b16 v[134:135], v226 offset:42240\n" \
    "s_waitcnt lgkmcnt(10)\n" \
    "v_mfma_f32_32x32x16_bf16 v[152:167], v[112:115], v[72:75], v[152:167]\n" \
    "ds_read_b64_tr_b16 v[112:113], v226 offset:6144\n" \
    "ds_read_b64_tr_b16 v[114:115], v226 offset:6400\n" \
    "s_waitcnt lgkmcnt(10)\n" \
    "v_mfma_f32_32x32x16_bf16 v[152:167], v[116:119], v[76:79], v[152:167]\n" \
    "ds_read_b64_tr_b16 v[116:117], v226 offset:18432\n" \
    "ds_read_b64_tr_b16 v[118:119], v226 offset:18688\n" \
    "s_waitcnt lgkmcnt(10)\n" \
    "v_mfma_f32_32x32x16_bf16 v[152:167], v[120:123], v[80:83], v[152:167]\n" \
    "ds_read_b64_tr_b16 v[120:121], v226 offset:30720\n" \
    "ds_read_b64_tr_b16 v[122:123], v226 offset:30976\n" \
    "s_waitcnt lgkmcnt(10)\n" \
    "v_mfma_f32_32x32x16_bf16 v[152:167], v[124:127], v[84:87], v[152:167]\n" \
    "ds_read_b64_tr_b16 v[124:125], v226 offset:43008\n" \
    "ds_read_b64_tr_b16 v[126:127], v226 offset:43264\n" \
    "s_waitcnt lgkmcnt(10)\n" \
    "v_mfma_f32_32x32x16_bf16 v[152:167], v[128:131], v[88:91], v[152:167]\n" \
    "s_cbranch_scc1 LNS_%=\n" \
    "buffer_load_dwordx4 v[136:139], v236, s[40:43], 0 offen offset:0 sc1\n" \
    "buffer_load_dwordx4 v[140:143], v206, s[40:43], 0 offen offset:0 sc1\n" \
    "s_mov_b64 exec, s[48:49]\n" \
    "buffer_load_dwordx4 v[168:171], v237, s[40:43], 0 offen offset:0 sc1\n" \
    "buffer_load_dwordx4 v[172:175], v207, s[40:43], 0 offen offset:0 sc1\n" \
    "s_mov_b64 exec, -1\n" \
    "buffer_load_dwordx4 v[144:147], v236, s[40:43], 0 offen offset:512 sc1\n" \
    "buffer_load_dwordx4 v[148:151], v206, s[40:43], 0 offen offset:512 sc1\n" \
    "LNS_%=:\n" \
    "s_waitcnt lgkmcnt(8)\n" \
    "v_mfma_f32_32x32x16_bf16 v[152:167], v[132:135], v[92:95], v[152:167]\n" \
    "s_waitcnt lgkmcnt(6)\n" \
    "v_mfma_f32_32x32x16_bf16 v[152:167], v[112:115], v[96:99], v[152:167]\n" \
    "s_waitcnt lgkmcnt(4)\n" \
    "v_mfma_f32_32x32x16_bf16 v[152:167], v[116:119], v[100:103], v[152:167]\n" \
    "s_waitcnt lgkmcnt(2)\n" \
    "v_mfma_f32_32x32x16_bf16 v[152:167], v[120:123], v[104:107], v[152:167]\n" \
    "s_waitcnt lgkmcnt(0)\n" \
    "v_mfma_f32_32x32x16_bf16 v[152:167], v[124:127], v[108:111], v[152:167]\n" \
    "s_nop 11\n" \
    "v_pk_mul_f32 v[152:153], v[152:153], s[46:47]\n" \
    "v_pk_mul_f32 v[154:155], v[154:155], s[46:47]\n" \
    "v_pk_mul_f32 v[156:157], v[156:157], s[46:47]\n" \
    "v_pk_mul_f32 v[158:159], v[158:159], s[46:47]\n" \
    "v_pk_mul_f32 v[160:161], v[160:161], s[46:47]\n" \
    "v_pk_mul_f32 v[162:163], v[162:163], s[46:47]\n" \
    "v_pk_mul_f32 v[164:165], v[164:165], s[46:47]\n" \
    "v_pk_mul_f32 v[166:167], v[166:167], s[46:47]\n" \
    "v_cvt_pk_bf16_f32 v88, v152, v153\n" \
    "v_cvt_pk_bf16_f32 v89, v154, v155\n" \
    "v_cvt_pk_bf16_f32 v90, v156, v157\n" \
    "v_cvt_pk_bf16_f32 v91, v158, v159\n" \
    "v_cvt_pk_bf16_f32 v92, v160, v161\n" \
    "v_cvt_pk_bf16_f32 v93, v162, v163\n" \
    "v_cvt_pk_bf16_f32 v94, v164, v165\n" \
    "v_cvt_pk_bf16_f32 v95, v166, v167\n" \
    "s_mov_b64 exec, s[84:85]\n" \
    "v_mov_b64 v[88:89], 0\n" \
    "s_mov_b64 exec, s[86:87]\n" \
    "v_mov_b64 v[94:95], 0\n" \
    "s_mov_b64 exec, -1\n" \
    "ds_write_b128 v227, v[88:91]\n" \
    "ds_write_b128 v227, v[92:95] offset:1024\n" \
    "s_mov_b64 exec, 1\n" \
    "ds_add_u32 v232, v203 offset:24\n" \
    "s_mov_b32 s45, 3\n" \
    "s_cbranch_scc1 LX_%=\n" \
    "s_waitcnt vmcnt(6)\n" \
    "ds_add_rtn_u32 v202, v232, v203 offset:28\n" \
    "s_waitcnt lgkmcnt(0)\n" \
    "v_cmp_eq_u32 vcc, 3, v202\n" \
    "s_cbranch_vccz LNQ_%=\n" \
    "s_mov_b64 exec, 15\n" \
    "v_and_b32 v205, -32, v205\n" \
    "v_lshl_add_u32 v205, %[ln], 3, v205\n" \
    "buffer_store_dwordx2 %[tag], v205, s[40:43], 0 offen sc1\n" \
    "s_mov_b64 exec, -1\n" \
    "s_waitcnt vmcnt(1)\n" \
    "s_branch LVA_%=\n" \
    "LNQ_%=:\n" \
    "s_mov_b64 exec, -1\n" \
    "s_waitcnt vmcnt(0)\n" \
    "LVA_%=:\n" \
    "v_max3_u32 v202, v136, v137, v138\n" \
    "v_max3_u32 v204, v139, v140, v141\n" \
    "v_max3_u32 v202, v202, v142, v143\n" \
    "v_max3_u32 v204, v204, v144, v145\n" \
    "v_max3_u32 v202, v202, v146, v147\n" \
    "v_max3_u32 v204, v204, v148, v149\n" \
    "v_max3_u32 v202, v202, v150, v151\n" \
    "v_max3_u32 v204, v204, v168, v169\n" \
    "v_max3_u32 v202, v202, v170, v171\n" \
    "v_max3_u32 v204, v204, v172, v173\n" \
    "v_max3_u32 v202, v202, v174, v175\n" \
    "v_max_u32 v202, v202, v204\n" \
    "v_cmp_eq_u32 vcc, -1, v202\n" \
    "s_cbranch_vccnz LBAD_%=\n" \
    "s_mov_b32 s45, 0\n" \
    "LA1_%=:\n" \
    "s_mov_b32 %[cnt], 0x200000\n" \
    "LSPR_%=:\n" \
    "ds_read_b32 v202, v232 offset:24\n" \
    "s_sub_u32 %[cnt], %[cnt], 1\n" \
    "s_cbranch_scc1 LGOR_%=\n" \
    "s_waitcnt lgkmcnt(0)\n" \
    "v_cmp_gt_u32 vcc, 4, v202\n" \
    "s_cbranch_vccnz LSPR_%=\n" \
    "LGOR_%=:\n" \
    "s_waitcnt lgkmcnt(0)\n" \
    "ds_read_b128 v[64:67], v228 offset:0\n" \
    "ds_read_b128 v[68:71], v228 offset:1024\n" \
    "ds_read_b128 v[72:75], v239 offset:0\n" \
    "ds_read_b128 v[76:79], v239 offset:1024\n" \
    "ds_read_b128 v[80:83], v235 offset:0\n" \
    "ds_read_b128 v[84:87], v235 offset:1024\n" \
    "s_setprio 1\n" \
    "v_mov_b64 v[208:209], -1\n" \
    "v_mov_b64 v[210:211], -1\n" \
    "v_mov_b32 v205, s81\n" \
    "s_waitcnt lgkmcnt(4)\n" \
    "v_mfma_f32_32x32x16_bf16 v[96:111], v[136:139], v[64:67], 0\n" \
    "v_mfma_f32_32x32x16_bf16 v[96:111], v[140:143], v[68:71], v[96:111]\n" \
    "s_mov_b64 exec, s[48:49]\n" \
    "buffer_load_dwordx4 v[176:179], v237, s[40:43], 0 offen offset:512 sc1\n" \
    "buffer_load_dwordx4 v[180:183], v207, s[40:43], 0 offen offset:512 sc1\n" \
    "s_mov_b64 exec, -1\n" \
    "buffer_load_dwordx4 v[152:155], v236, s[40:43], 0 offen offset:1024 sc1\n" \
    "buffer_load_dwordx4 v[156:159], v206, s[40:43], 0 offen offset:1024 sc1\n" \
    "s_mov_b64 exec, s[48:49]\n" \
    "buffer_load_dwordx4 v[184:187], v237, s[40:43], 0 offen offset:1024 sc1\n" \
    "buffer_load_dwordx4 v[188:191], v207, s[40:43], 0 offen offset:1024 sc1\n" \
    "s_mov_b64 exec, -1\n" \
    "buffer_load_dwordx4 v[160:163], v236, s[40:43], 0 offen offset:1536 sc1\n" \
    "buffer_load_dwordx4 v[164:167], v206, s[40:43], 0 offen offset:1536 sc1\n" \
    "s_waitcnt lgkmcnt(0)\n" \
    "v_exp_f32 v96, v96\n" \
    "v_exp_f32 v97, v97\n" \
    "v_exp_f32 v98, v98\n" \
    "v_exp_f32 v99, v99\n" \
    "v_mfma_f32_32x32x16_bf16 v[112:127], v[136:139], v[72:75], 0\n" \
    "s_mov_b64 exec, s[48:49]\n" \
    "buffer_load_dwordx4 v[192:195], v237, s[40:43], 0 offen offset:1536 sc1\n" \
    "buffer_load_dwordx4 v[196:199], v207, s[40:43], 0 offen offset:1536 sc1\n" \
    "s_mov_b64 exec, -1\n" \
    "v_exp_f32 v100, v100\n" \
    "v_exp_f32 v101, v101\n" \
    "v_cvt_pk_bf16_f32 v128, v96, v97\n" \
    "v_cvt_pk_bf16_f32 v129, v98, v99\n" \
    "v_exp_f32 v102, v102\n" \
    "v_exp_f32 v103, v103\n" \
    "v_mfma_f32_32x32x16_bf16 v[112:127], v[140:143], v[76:79], v[112:127]\n" \
    "s_mov_b64 exec, 1\n" \
    "ds_add_u32 v232, v203 offset:80\n" \
    "s_mov_b64 exec, -1\n" \
    "v_exp_f32 v104, v104\n" \
    "v_exp_f32 v105, v105\n" \
    "v_cvt_pk_bf16_f32 v130, v100, v101\n" \
    "v_cvt_pk_bf16_f32 v131, v102, v103\n" \
    "v_exp_f32 v106, v106\n" \
    "v_exp_f32 v107, v107\n" \
    "v_exp_f32 v108, v108\n" \
    "v_exp_f32 v109, v109\n" \
    "v_cvt_pk_bf16_f32 v132, v104, v105\n" \
    "v_cvt_pk_bf16_f32 v133, v106, v107\n" \
    "v_exp_f32 v110, v110\n" \
    "v_exp_f32 v111, v111\n" \
    "v_mfma_f32_32x32x16_bf16 v[0:15], v[168:171], v[128:131], 0\n" \
    "v_exp_f32 v112, v112\n" \
    "v_exp_f32 v113, v113\n" \
    "v_cvt_pk_bf16_f32 v134, v108, v109\n" \
    "v_cvt_pk_bf16_f32 v135, v110, v111\n" \
    "v_exp_f32 v114, v114\n" \
    "v_exp_f32 v115, v115\n" \
    "v_mfma_f32_32x32x16_bf16 v[96:111], v[136:139], v[80:83], 0\n" \
    "v_exp_f32 v116, v116\n" \
    "v_exp_f32 v117, v117\n" \
    "v_cvt_pk_bf16_f32 v128, v112, v113\n" \
    "v_cvt_pk_bf16_f32 v129, v114, v115\n" \
    "v_exp_f32 v118, v118\n" \
    "v_exp_f32 v119, v119\n" \
    "v_mfma_f32_32x32x16_bf16 v[96:111], v[140:143], v[84:87], v[96:111]\n" \
    "v_exp_f32 v120, v120\n" \
    "v_exp_f32 v121, v121\n" \
    "v_cvt_pk_bf16_f32 v130, v116, v117\n" \
    "v_cvt_pk_bf16_f32 v131, v118, v119\n" \
    "v_exp_f32 v122, v122\n" \
    "v_exp_f32 v123, v123\n" \
    "v_mfma_f32_32x32x16_bf16 v[0:15], v[172:175], v[132:135], v[0:15]\n" \
    "v_exp_f32 v124, v124\n" \
    "v_exp_f32 v125, v125\n" \
    "v_cvt_pk_bf16_f32 v132, v120, v121\n" \
    "v_cvt_pk_bf16_f32 v133, v122, v123\n" \
    "v_exp_f32 v126, v126\n" \
    "v_exp_f32 v127, v127\n" \
    "v_mfma_f32_32x32x16_bf16 v[16:31], v[168:171], v[128:131], 0\n" \
    "v_exp_f32 v96, v96\n" \
    "v_exp_f32 v97, v97\n" \
    "v_cvt_pk_bf16_f32 v134, v124, v125\n" \
    "v_cvt_pk_bf16_f32 v135, v126, v127\n" \
    "v_exp_f32 v98, v98\n" \
    "v_exp_f32 v99, v99\n" \
    "v_mfma_f32_32x32x16_bf16 v[112:127], v[136:139], v[88:91], 0\n" \
    "v_exp_f32 v100, v100\n" \
    "v_exp_f32 v101, v101\n" \
    "v_cvt_pk_bf16_f32 v128, v96, v97\n" \
    "v_cvt_pk_bf16_f32 v129, v98, v99\n" \
    "v_exp_f32 v102, v102\n" \
    "v_exp_f32 v103, v103\n" \
    "v_mfma_f32_32x32x16_bf16 v[112:127], v[140:143], v[92:95], v[112:127]\n" \
    "v_exp_f32 v104, v104\n" \
    "v_exp_f32 v105, v105\n" \
    "v_cvt_pk_bf16_f32 v130, v100, v101\n" \
    "v_cvt_pk_bf16_f32 v131, v102, v103\n" \
    "v_exp_f32 v106, v106\n" \
    "v_exp_f32 v107, v107\n" \
    "v_mfma_f32_32x32x16_bf16 v[16:31], v[172:175], v[132:135], v[16:31]\n" \
    "v_exp_f32 v108, v108\n" \
    "v_exp_f32 v109, v109\n" \
    "v_cvt_pk_bf16_f32 v132, v104, v105\n" \
    "v_cvt_pk_bf16_f32 v133, v106, v107\n" \
    "v_exp_f32 v110, v110\n" \
    "v_exp_f32 v111, v111\n" \
    "v_mfma_f32_32x32x16_bf16 v[32:47], v[168:171], v[128:131], 0\n" \
    "v_exp_f32 v112, v112\n" \
    "v_exp_f32 v113, v113\n" \
    "v_cvt_pk_bf16_f32 v134, v108, v109\n" \
    "v_cvt_pk_bf16_f32 v135, v110, v111\n" \
    "v_exp_f32 v114, v114\n" \
    "v_exp_f32 v115, v115\n" \
    "v_mfma_f32_32x32x16_bf16 v[96:111], v[144:147], v[64:67], 0\n" \
    "v_exp_f32 v116, v116\n" \
    "v_exp_f32 v117, v117\n" \
    "v_cvt_pk_bf16_f32 v128, v112, v113\n" \
    "v_cvt_pk_bf16_f32 v129, v114, v115\n" \
    "v_exp_f32 v118, v118\n" \
    "v_exp_f32 v119, v119\n" \
    "v_mfma_f32_32x32x16_bf16 v[96:111], v[148:151], v[68:71], v[96:111]\n" \
    "v_exp_f32 v120, v120\n" \
    "v_exp_f32 v121, v121\n" \
    "v_cvt_pk_bf16_f32 v130, v116, v117\n" \
    "v_cvt_pk_bf16_f32 v131, v118, v119\n" \
    "v_exp_f32 v122, v122\n" \
    "v_exp_f32 v123, v123\n" \
    "v_mfma_f32_32x32x16_bf16 v[32:47], v[172:175], v[132:135], v[32:47]\n" \
    "v_exp_f32 v124, v124\n" \
    "v_exp_f32 v125, v125\n" \
    "v_cvt_pk_bf16_f32 v132, v120, v121\n" \
    "v_cvt_pk_bf16_f32 v133, v122, v123\n" \
    "v_exp_f32 v126, v126\n" \
    "v_exp_f32 v127, v127\n" \
    "v_mfma_f32_32x32x16_bf16 v[48:63], v[168:171], v[128:131], 0\n" \
    "v_exp_f32 v96, v96\n" \
    "v_exp_f32 v97, v97\n" \
    "v_cvt_pk_bf16_f32 v134, v124, v125\n" \
    "v_cvt_pk_bf16_f32 v135, v126, v127\n" \
    "v_exp_f32 v98, v98\n" \
    "v_exp_f32 v99, v99\n" \
    "v_mfma_f32_32x32x16_bf16 v[112:127], v[144:147], v[72:75], 0\n" \
    "v_exp_f32 v100, v100\n" \
    "v_exp_f32 v101, v101\n" \
    "v_cvt_pk_bf16_f32 v128, v96, v97\n" \
    "v_cvt_pk_bf16_f32 v129, v98, v99\n" \
    "v_exp_f32 v102, v102\n" \
    "v_exp_f32 v103, v103\n" \
    "v_mfma_f32_32x32x16_bf16 v[112:127], v[148:151], v[76:79], v[112:127]\n" \
    "s_waitcnt vmcnt(8)\n" \
    "v_exp_f32 v104, v104\n" \
    "v_exp_f32 v105, v105\n" \
    "v_cvt_pk_bf16_f32 v130, v100, v101\n" \
    "v_cvt_pk_bf16_f32 v131, v102, v103\n" \
    "v_exp_f32 v106, v106\n" \
    "v_exp_f32 v107, v107\n" \
    "v_mfma_f32_32x32x16_bf16 v[48:63], v[172:175], v[132:135], v[48:63]\n" \
    "v_exp_f32 v108, v108\n" \
    "v_exp_f32 v109, v109\n" \
    "v_cvt_pk_bf16_f32 v132, v104, v105\n" \
    "v_cvt_pk_bf16_f32 v133, v106, v107\n" \
    "v_exp_f32 v110, v110\n" \
    "v_exp_f32 v111, v111\n" \
    "v_mfma_f32_32x32x16_bf16 v[0:15], v[176:179], v[128:131], v[0:15]\n" \
    "v_exp_f32 v112, v112\n" \
    "v_exp_f32 v113, v113\n" \
    "v_cvt_pk_bf16_f32 v134, v108, v109\n" \
    "v_cvt_pk_bf16_f32 v135, v110, v111\n" \
    "v_exp_f32 v114, v114\n" \
    "v_exp_f32 v115, v115\n" \
    "v_mfma_f32_32x32x16_bf16 v[96:111], v[144:147], v[80:83], 0\n" \
    "v_exp_f32 v116, v116\n" \
    "v_exp_f32 v117, v117\n" \
    "v_cvt_pk_bf16_f32 v128, v112, v113\n" \
    "v_cvt_pk_bf16_f32 v129, v114, v115\n" \
    "v_exp_f32 v118, v118\n" \
    "v_exp_f32 v119, v119\n" \
    "v_mfma_f32_32x32x16_bf16 v[96:111], v[148:151], v[84:87], v[96:111]\n" \
    "v_exp_f32 v120, v120\n" \
    "v_exp_f32 v121, v121\n" \
    "v_cvt_pk_bf16_f32 v130, v116, v117\n" \
    "v_cvt_pk_bf16_f32 v131, v118, v119\n" \
    "v_exp_f32 v122, v122\n" \
    "v_exp_f32 v123, v123\n" \
    "v_mfma_f32_32x32x16_bf16 v[0:15], v[180:183], v[132:135], v[0:15]\n" \
    "v_exp_f32 v124, v124\n" \
    "v_exp_f32 v125, v125\n" \
    "v_cvt_pk_bf16_f32 v132, v120, v121\n" \
    "v_cvt_pk_bf16_f32 v133, v122, v123\n" \
    "v_exp_f32 v126, v126\n" \
    "v_exp_f32 v127, v127\n" \
    "v_mfma_f32_32x32x16_bf16 v[16:31], v[176:179], v[128:131], v[16:31]\n" \
    "v_exp_f32 v96, v96\n" \
    "v_exp_f32 v97, v97\n" \
    "v_cvt_pk_bf16_f32 v134, v124, v125\n" \
    "v_cvt_pk_bf16_f32 v135, v126, v127\n" \
    "v_exp_f32 v98, v98\n" \
    "v_exp_f32 v99, v99\n" \
    "v_mfma_f32_32x32x16_bf16 v[112:127], v[144:147], v[88:91], 0\n" \
    "v_exp_f32 v100, v100\n" \
    "v_exp_f32 v101, v101\n" \
    "v_cvt_pk_bf16_f32 v128, v96, v97\n" \
    "v_cvt_pk_bf16_f32 v129, v98, v99\n" \
    "v_exp_f32 v102, v102\n" \
    "v_exp_f32 v103, v103\n" \
    "v_mfma_f32_32x32x16_bf16 v[112:127], v[148:151], v[92:95], v[112:127]\n" \
    "v_exp_f32 v104, v104\n" \
    "v_exp_f32 v105, v105\n" \
    "v_cvt_pk_bf16_f32 v130, v100, v101\n" \
    "v_cvt_pk_bf16_f32 v131, v102, v103\n" \
    "v_exp_f32 v106, v106\n" \
    "v_exp_f32 v107, v107\n" \
    "v_mfma_f32_32x32x16_bf16 v[16:31], v[180:183], v[132:135], v[16:31]\n" \
    "v_exp_f32 v108, v108\n" \
    "v_exp_f32 v109, v109\n" \
    "v_cvt_pk_bf16_f32 v132, v104, v105\n" \
    "v_cvt_pk_bf16_f32 v133, v106, v107\n" \
    "v_exp_f32 v110, v110\n" \
    "v_exp_f32 v111, v111\n" \
    "v_mfma_f32_32x32x16_bf16 v[32:47], v[176:179], v[128:131], v[32:47]\n" \
    "s_waitcnt vmcnt(6)\n" \
    "v_exp_f32 v112, v112\n" \
    "v_exp_f32 v113, v113\n" \
    "v_cvt_pk_bf16_f32 v134, v108, v109\n" \
    "v_cvt_pk_bf16_f32 v135, v110, v111\n" \
    "v_exp_f32 v114, v114\n" \
    "v_exp_f32 v115, v115\n" \
    "v_mfma_f32_32x32x16_bf16 v[96:111], v[152:155], v[64:67], 0\n" \
    "buffer_load_dwordx2 v[200:201], v238, s[40:43], 0 offen sc1\n" \
    "v_exp_f32 v116, v116\n" \
    "v_exp_f32 v117, v117\n" \
    "v_cvt_pk_bf16_f32 v128, v112, v113\n" \
    "v_cvt_pk_bf16_f32 v129, v114, v115\n" \
    "v_exp_f32 v118, v118\n" \
    "v_exp_f32 v119, v119\n" \
    "v_mfma_f32_32x32x16_bf16 v[96:111], v[156:159], v[68:71], v[96:111]\n" \
    "v_exp_f32 v120, v120\n" \
    "v_exp_f32 v121, v121\n" \
    "v_cvt_pk_bf16_f32 v130, v116, v117\n" \
    "v_cvt_pk_bf16_f32 v131, v118, v119\n" \
    "v_exp_f32 v122, v122\n" \
    "v_exp_f32 v123, v123\n" \
    "v_mfma_f32_32x32x16_bf16 v[32:47], v[180:183], v[132:135], v[32:47]\n" \
    "v_exp_f32 v124, v124\n" \
    "v_exp_f32 v125, v125\n" \
    "v_cvt_pk_bf16_f32 v132, v120, v121\n" \
    "v_cvt_pk_bf16_f32 v133, v122, v123\n" \
    "v_exp_f32 v126, v126\n" \
    "v_exp_f32 v127, v127\n" \
    "v_mfma_f32_32x32x16_bf16 v[48:63], v[176:179], v[128:131], v[48:63]\n" \
    "v_exp_f32 v96, v96\n" \
    "v_exp_f32 v97, v97\n" \
    "v_cvt_pk_bf16_f32 v134, v124, v125\n" \
    "v_cvt_pk_bf16_f32 v135, v126, v127\n" \
    "v_exp_f32 v98, v98\n" \
    "v_exp_f32 v99, v99\n" \
    "v_mfma_f32_32x32x16_bf16 v[112:127], v[152:155], v[72:75], 0\n" \
    "v_exp_f32 v100, v100\n" \
    "v_exp_f32 v101, v101\n" \
    "v_cvt_pk_bf16_f32 v128, v96, v97\n" \
    "v_cvt_pk_bf16_f32 v129, v98, v99\n" \
    "v_exp_f32 v102, v102\n" \
    "v_exp_f32 v103, v103\n" \
    "v_mfma_f32_32x32x16_bf16 v[112:127], v[156:159], v[76:79], v[112:127]\n" \
    "s_waitcnt vmcnt(5)\n" \
    "v_exp_f32 v104, v104\n" \
    "v_exp_f32 v105, v105\n" \
    "v_cvt_pk_bf16_f32 v130, v100, v101\n" \
    "v_cvt_pk_bf16_f32 v131, v102, v103\n" \
    "v_exp_f32 v106, v106\n" \
    "v_exp_f32 v107, v107\n" \
    "v_mfma_f32_32x32x16_bf16 v[48:63], v[180:183], v[132:135], v[48:63]\n" \
    "v_exp_f32 v108, v108\n" \
    "v_exp_f32 v109, v109\n" \
    "v_cvt_pk_bf16_f32 v132, v104, v105\n" \
    "v_cvt_pk_bf16_f32 v133, v106, v107\n" \
    "v_exp_f32 v110, v110\n" \
    "v_exp_f32 v111, v111\n" \
    "v_mfma_f32_32x32x16_bf16 v[0:15], v[184:187], v[128:131], v[0:15]\n" \
    "v_exp_f32 v112, v112\n" \
    "v_exp_f32 v113, v113\n" \
    "v_cvt_pk_bf16_f32 v134, v108, v109\n" \
    "v_cvt_pk_bf16_f32 v135, v110, v111\n" \
    "v_exp_f32 v114, v114\n" \
    "v_exp_f32 v115, v115\n" \
    "v_mfma_f32_32x32x16_bf16 v[96:111], v[152:155], v[80:83], 0\n" \
    "v_exp_f32 v116, v116\n" \
    "v_exp_f32 v117, v117\n" \
    "v_cvt_pk_bf16_f32 v128, v112, v113\n" \
    "v_cvt_pk_bf16_f32 v129, v114, v115\n" \
    "v_exp_f32 v118, v118\n" \
    "v_exp_f32 v119, v119\n" \
    "v_mfma_f32_32x32x16_bf16 v[96:111], v[156:159], v[84:87], v[96:111]\n" \
    "v_exp_f32 v120, v120\n" \
    "v_exp_f32 v121, v121\n" \
    "v_cvt_pk_bf16_f32 v130, v116, v117\n" \
    "v_cvt_pk_bf16_f32 v131, v118, v119\n" \
    "v_exp_f32 v122, v122\n" \
    "v_exp_f32 v123, v123\n" \
    "v_mfma_f32_32x32x16_bf16 v[0:15], v[188:191], v[132:135], v[0:15]\n" \
    "v_exp_f32 v124, v124\n" \
    "v_exp_f32 v125, v125\n" \
    "v_cvt_pk_bf16_f32 v132, v120, v121\n" \
    "v_cvt_pk_bf16_f32 v133, v122, v123\n" \
    "v_exp_f32 v126, v126\n" \
    "v_exp_f32 v127, v127\n" \
    "v_mfma_f32_32x32x16_bf16 v[16:31], v[184:187], v[128:131], v[16:31]\n" \
    "v_exp_f32 v96, v96\n" \
    "v_exp_f32 v97, v97\n" \
    "v_cvt_pk_bf16_f32 v134, v124, v125\n" \
    "v_cvt_pk_bf16_f32 v135, v126, v127\n" \
    "v_exp_f32 v98, v98\n" \
    "v_exp_f32 v99, v99\n" \
    "v_mfma_f32_32x32x16_bf16 v[112:127], v[152:155], v[88:91], 0\n" \
    "v_exp_f32 v100, v100\n" \
    "v_exp_f32 v101, v101\n" \
    "v_cvt_pk_bf16_f32 v128, v96, v97\n" \
    "v_cvt_pk_bf16_f32 v129, v98, v99\n" \
    "v_exp_f32 v102, v102\n" \
    "v_exp_f32 v103, v103\n" \
    "v_mfma_f32_32x32x16_bf16 v[112:127], v[156:159], v[92:95], v[112:127]\n" \
    "v_exp_f32 v104, v104\n" \
    "v_exp_f32 v105, v105\n" \
    "v_cvt_pk_bf16_f32 v130, v100, v101\n" \
    "v_cvt_pk_bf16_f32 v131, v102, v103\n" \
    "v_exp_f32 v106, v106\n" \
    "v_exp_f32 v107, v107\n" \
    "v_mfma_f32_32x32x16_bf16 v[16:31], v[188:191], v[132:135], v[16:31]\n" \
    "v_exp_f32 v108, v108\n" \
    "v_exp_f32 v109, v109\n" \
    "v_cvt_pk_bf16_f32 v132, v104, v105\n" \
    "v_cvt_pk_bf16_f32 v133, v106, v107\n" \
    "v_exp_f32 v110, v110\n" \
    "v_exp_f32 v111, v111\n" \
    "v_mfma_f32_32x32x16_bf16 v[32:47], v[184:187], v[128:131], v[32:47]\n" \
    "s_cmp_eq_u32 s45, 0\n" \
    "s_cbranch_scc0 LUP_%=\n" \
    "s_waitcnt vmcnt(0)\n" \
    "v_max3_u32 v202, v152, v153, v154\n" \
    "v_max3_u32 v204, v155, v156, v157\n" \
    "v_max3_u32 v202, v202, v158, v159\n" \
    "v_max3_u32 v204, v204, v160, v161\n" \
    "v_max3_u32 v202, v202, v162, v163\n" \
    "v_max3_u32 v204, v204, v164, v165\n" \
    "v_max3_u32 v202, v202, v166, v167\n" \
    "v_max3_u32 v204, v204, v176, v177\n" \
    "v_max3_u32 v202, v202, v178, v179\n" \
    "v_max3_u32 v204, v204, v180, v181\n" \
    "v_max3_u32 v202, v202, v182, v183\n" \
    "v_max3_u32 v204, v204, v184, v185\n" \
    "v_max3_u32 v202, v202, v186, v187\n" \
    "v_max3_u32 v204, v204, v188, v189\n" \
    "v_max3_u32 v202, v202, v190, v191\n" \
    "v_max3_u32 v204, v204, v192, v193\n" \
    "v_max3_u32 v202, v202, v194, v195\n" \
    "v_max3_u32 v204, v204, v196, v197\n" \
    "v_max3_u32 v202, v202, v198, v199\n" \
    "v_max_u32 v202, v202, v204\n" \
    "v_cmp_eq_u32 vcc, -1, v202\n" \
    "s_cbranch_vccnz LFB_%=\n" \
    "v_cmp_ne_u64 vcc, v[200:201], %[tag]\n" \
    "s_cbranch_vccnz LMID_%=\n" \
    "LUP_%=:\n" \
    "s_waitcnt vmcnt(3)\n" \
    "v_exp_f32 v112, v112\n" \
    "v_exp_f32 v113, v113\n" \
    "v_cvt_pk_bf16_f32 v134, v108, v109\n" \
    "v_cvt_pk_bf16_f32 v135, v110, v111\n" \
    "v_exp_f32 v114, v114\n" \
    "v_exp_f32 v115, v115\n" \
    "v_mfma_f32_32x32x16_bf16 v[96:111], v[160:163], v[64:67], 0\n" \
    "buffer_load_dwordx4 v[136:139], v236, s[40:43], 0 offen offset:2048 sc1\n" \
    "v_exp_f32 v116, v116\n" \
    "v_exp_f32 v117, v117\n" \
    "v_cvt_pk_bf16_f32 v128, v112, v113\n" \
    "v_cvt_pk_bf16_f32 v129, v114, v115\n" \
    "v_exp_f32 v118, v118\n" \
    "v_exp_f32 v119, v119\n" \
    "v_mfma_f32_32x32x16_bf16 v[96:111], v[164:167], v[68:71], v[96:111]\n" \
    "buffer_load_dwordx4 v[140:143], v206, s[40:43], 0 offen offset:2048 sc1\n" \
    "v_exp_f32 v120, v120\n" \
    "v_exp_f32 v121, v121\n" \
    "v_cvt_pk_bf16_f32 v130, v116, v117\n" \
    "v_cvt_pk_bf16_f32 v131, v118, v119\n" \
    "v_exp_f32 v122, v122\n" \
    "v_exp_f32 v123, v123\n" \
    "v_mfma_f32_32x32x16_bf16 v[32:47], v[188:191], v[132:135], v[32:47]\n" \
    "s_mov_b64 exec, s[48:49]\n" \
    "buffer_load_dwordx4 v[168:171], v237, s[40:43], 0 offen offset:2048 sc1\n" \
    "buffer_load_dwordx4 v[172:175], v207, s[40:43], 0 offen offset:2048 sc1\n" \
    "buffer_load_dwordx4 v[176:179], v237, s[40:43], 0 offen offset:2560 sc1\n" \
    "buffer_load_dwordx4 v[180:183], v207, s[40:43], 0 offen offset:2560 sc1\n" \
    "s_mov_b64 exec, -1\n" \
    "v_exp_f32 v124, v124\n" \
    "v_exp_f32 v125, v125\n" \
    "v_cvt_pk_bf16_f32 v132, v120, v121\n" \
    "v_cvt_pk_bf16_f32 v133, v122, v123\n" \
    "v_exp_f32 v126, v126\n" \
    "v_exp_f32 v127, v127\n" \
    "v_mfma_f32_32x32x16_bf16 v[48:63], v[184:187], v[128:131], v[48:63]\n" \
    "buffer_load_dwordx4 v[144:147], v236, s[40:43], 0 offen offset:2560 sc1\n" \
    "v_exp_f32 v96, v96\n" \
    "v_exp_f32 v97, v97\n" \
    "v_cvt_pk_bf16_f32 v134, v124, v125\n" \
    "v_cvt_pk_bf16_f32 v135, v126, v127\n" \
    "v_exp_f32 v98, v98\n" \
    "v_exp_f32 v99, v99\n" \
    "v_mfma_f32_32x32x16_bf16 v[112:127], v[160:163], v[72:75], 0\n" \
    "buffer_load_dwordx4 v[148:151], v206, s[40:43], 0 offen offset:2560 sc1\n" \
    "v_exp_f32 v100, v100\n" \
    "v_exp_f32 v101, v101\n" \
    "v_cvt_pk_bf16_f32 v128, v96, v97\n" \
    "v_cvt_pk_bf16_f32 v129, v98, v99\n" \
    "v_exp_f32 v102, v102\n" \
    "v_exp_f32 v103, v103\n" \
    "v_mfma_f32_32x32x16_bf16 v[112:127], v[164:167], v[76:79], v[112:127]\n" \
    "s_waitcnt vmcnt(9)\n" \
    "v_exp_f32 v104, v104\n" \
    "v_exp_f32 v105, v105\n" \
    "v_cvt_pk_bf16_f32 v130, v100, v101\n" \
    "v_cvt_pk_bf16_f32 v131, v102, v103\n" \
    "v_exp_f32 v106, v106\n" \
    "v_exp_f32 v107, v107\n" \
    "v_mfma_f32_32x32x16_bf16 v[48:63], v[188:191], v[132:135], v[48:63]\n" \
    "buffer_load_dwordx4 v[152:155], v236, s[40:43], 0 offen offset:3072 sc1\n" \
    "v_exp_f32 v108, v108\n" \
    "v_exp_f32 v109, v109\n" \
    "v_cvt_pk_bf16_f32 v132, v104, v105\n" \
    "v_cvt_pk_bf16_f32 v133, v106, v107\n" \
    "v_exp_f32 v110, v110\n" \
    "v_exp_f32 v111, v111\n" \
    "v_mfma_f32_32x32x16_bf16 v[0:15], v[192:195], v[128:131], v[0:15]\n" \
    "buffer_load_dwordx4 v[156:159], v206, s[40:43], 0 offen offset:3072 sc1\n" \
    "v_exp_f32 v112, v112\n" \
    "v_exp_f32 v113, v113\n" \
    "v_cvt_pk_bf16_f32 v134, v108, v109\n" \
    "v_cvt_pk_bf16_f32 v135, v110, v111\n" \
    "v_exp_f32 v114, v114\n" \
    "v_exp_f32 v115, v115\n" \
    "v_mfma_f32_32x32x16_bf16 v[96:111], v[160:163], v[80:83], 0\n" \
    "v_exp_f32 v116, v116\n" \
    "v_exp_f32 v117, v117\n" \
    "v_cvt_pk_bf16_f32 v128, v112, v113\n" \
    "v_cvt_pk_bf16_f32 v129, v114, v115\n" \
    "v_exp_f32 v118, v118\n" \
    "v_exp_f32 v119, v119\n" \
    "v_mfma_f32_32x32x16_bf16 v[96:111], v[164:167], v[84:87], v[96:111]\n" \
    "v_exp_f32 v120, v120\n" \
    "v_exp_f32 v121, v121\n" \
    "v_cvt_pk_bf16_f32 v130, v116, v117\n" \
    "v_cvt_pk_bf16_f32 v131, v118, v119\n" \
    "v_exp_f32 v122, v122\n" \
    "v_exp_f32 v123, v123\n" \
    "v_mfma_f32_32x32x16_bf16 v[0:15], v[196:199], v[132:135], v[0:15]\n" \
    "v_exp_f32 v124, v124\n" \
    "v_exp_f32 v125, v125\n" \
    "v_cvt_pk_bf16_f32 v132, v120, v121\n" \
    "v_cvt_pk_bf16_f32 v133, v122, v123\n" \
    "v_exp_f32 v126, v126\n" \
    "v_exp_f32 v127, v127\n" \
    "v_mfma_f32_32x32x16_bf16 v[16:31], v[192:195], v[128:131], v[16:31]\n" \
    "v_exp_f32 v96, v96\n" \
    "v_exp_f32 v97, v97\n" \
    "v_cvt_pk_bf16_f32 v134, v124, v125\n" \
    "v_cvt_pk_bf16_f32 v135, v126, v127\n" \
    "v_exp_f32 v98, v98\n" \
    "v_exp_f32 v99, v99\n" \
    "v_mfma_f32_32x32x16_bf16 v[112:127], v[160:163], v[88:91], 0\n" \
    "buffer_load_dwordx4 v[160:163], v236, s[40:43], 0 offen offset:3584 sc1\n" \
    "v_exp_f32 v100, v100\n" \
    "v_exp_f32 v101, v101\n" \
    "v_cvt_pk_bf16_f32 v128, v96, v97\n" \
    "v_cvt_pk_bf16_f32 v129, v98, v99\n" \
    "v_exp_f32 v102, v102\n" \
    "v_exp_f32 v103, v103\n" \
    "v_mfma_f32_32x32x16_bf16 v[112:127], v[164:167], v[92:95], v[112:127]\n" \
    "buffer_load_dwordx4 v[164:167], v206, s[40:43], 0 offen offset:3584 sc1\n" \
    "v_exp_f32 v104, v104\n" \
    "v_exp_f32 v105, v105\n" \
    "v_cvt_pk_bf16_f32 v130, v100, v101\n" \
    "v_cvt_pk_bf16_f32 v131, v102, v103\n" \
    "v_exp_f32 v106, v106\n" \
    "v_exp_f32 v107, v107\n" \
    "v_mfma_f32_32x32x16_bf16 v[16:31], v[196:199], v[132:135], v[16:31]\n" \
    "v_exp_f32 v108, v108\n" \
    "v_exp_f32 v109, v109\n" \
    "v_cvt_pk_bf16_f32 v132, v104, v105\n" \
    "v_cvt_pk_bf16_f32 v133, v106, v107\n" \
    "v_exp_f32 v110, v110\n" \
    "v_exp_f32 v111, v111\n" \
    "v_mfma_f32_32x32x16_bf16 v[32:47], v[192:195], v[128:131], v[32:47]\n" \
    "s_waitcnt vmcnt(10)\n" \
    "v_exp_f32 v112, v112\n" \
    "v_exp_f32 v113, v113\n" \
    "v_cvt_pk_bf16_f32 v134, v108, v109\n" \
    "v_cvt_pk_bf16_f32 v135, v110, v111\n" \
    "v_exp_f32 v114, v114\n" \
    "v_exp_f32 v115, v115\n" \
    "v_mfma_f32_32x32x16_bf16 v[96:111], v[136:139], v[64:67], 0\n" \
    "buffer_store_dwordx4 v[208:211], v234, s[40:43], 0 offen offset:0 sc1\n" \
    "v_exp_f32 v116, v116\n" \
    "v_exp_f32 v117, v117\n" \
    "v_cvt_pk_bf16_f32 v128, v112, v113\n" \
    "v_cvt_pk_bf16_f32 v129, v114, v115\n" \
    "v_exp_f32 v118, v118\n" \
    "v_exp_f32 v119, v119\n" \
    "v_mfma_f32_32x32x16_bf16 v[96:111], v[140:143], v[68:71], v[96:111]\n" \
    "buffer_store_dwordx4 v[208:211], v234, s[40:43], 0 offen offset:1024 sc1\n" \
    "v_exp_f32 v120, v120\n" \
    "v_exp_f32 v121, v121\n" \
    "v_cvt_pk_bf16_f32 v130, v116, v117\n" \
    "v_cvt_pk_bf16_f32 v131, v118, v119\n" \
    "v_exp_f32 v122, v122\n" \
    "v_exp_f32 v123, v123\n" \
    "v_mfma_f32_32x32x16_bf16 v[32:47], v[196:199], v[132:135], v[32:47]\n" \
    "v_exp_f32 v124, v124\n" \
    "v_exp_f32 v125, v125\n" \
    "v_cvt_pk_bf16_f32 v132, v120, v121\n" \
    "v_cvt_pk_bf16_f32 v133, v122, v123\n" \
    "v_exp_f32 v126, v126\n" \
    "v_exp_f32 v127, v127\n" \
    "v_mfma_f32_32x32x16_bf16 v[48:63], v[192:195], v[128:131], v[48:63]\n" \
    "v_exp_f32 v96, v96\n" \
    "v_exp_f32 v97, v97\n" \
    "v_cvt_pk_bf16_f32 v134, v124, v125\n" \
    "v_cvt_pk_bf16_f32 v135, v126, v127\n" \
    "v_exp_f32 v98, v98\n" \
    "v_exp_f32 v99, v99\n" \
    "v_mfma_f32_32x32x16_bf16 v[112:127], v[136:139], v[72:75], 0\n" \
    "v_exp_f32 v100, v100\n" \
    "v_exp_f32 v101, v101\n" \
    "v_cvt_pk_bf16_f32 v128, v96, v97\n" \
    "v_cvt_pk_bf16_f32 v129, v98, v99\n" \
    "v_exp_f32 v102, v102\n" \
    "v_exp_f32 v103, v103\n" \
    "v_mfma_f32_32x32x16_bf16 v[112:127], v[140:143], v[76:79], v[112:127]\n" \
    "s_waitcnt vmcnt(10)\n" \
    "v_exp_f32 v104, v104\n" \
    "v_exp_f32 v105, v105\n" \
    "v_cvt_pk_bf16_f32 v130, v100, v101\n" \
    "v_cvt_pk_bf16_f32 v131, v102, v103\n" \
    "v_exp_f32 v106, v106\n" \
    "v_exp_f32 v107, v107\n" \
    "v_mfma_f32_32x32x16_bf16 v[48:63], v[196:199], v[132:135], v[48:63]\n" \
    "s_mov_b64 exec, s[48:49]\n" \
    "buffer_load_dwordx4 v[184:187], v237, s[40:43], 0 offen offset:3072 sc1\n" \
    "buffer_load_dwordx4 v[188:191], v207, s[40:43], 0 offen offset:3072 sc1\n" \
    "buffer_load_dwordx4 v[192:195], v237, s[40:43], 0 offen offset:3584 sc1\n" \
    "buffer_load_dwordx4 v[196:199], v207, s[40:43], 0 offen offset:3584 sc1\n" \
    "s_mov_b64 exec, -1\n" \
    "v_exp_f32 v108, v108\n" \
    "v_exp_f32 v109, v109\n" \
    "v_cvt_pk_bf16_f32 v132, v104, v105\n" \
    "v_cvt_pk_bf16_f32 v133, v106, v107\n" \
    "v_exp_f32 v110, v110\n" \
    "v_exp_f32 v111, v111\n" \
    "v_mfma_f32_32x32x16_bf16 v[0:15], v[168:171], v[128:131], v[0:15]\n" \
    "v_exp_f32 v112, v112\n" \
    "v_exp_f32 v113, v113\n" \
    "v_cvt_pk_bf16_f32 v134, v108, v109\n" \
    "v_cvt_pk_bf16_f32 v135, v110, v111\n" \
    "v_exp_f32 v114, v114\n" \
    "v_exp_f32 v115, v115\n" \
    "v_mfma_f32_32x32x16_bf16 v[96:111], v[136:139], v[80:83], 0\n" \
    "v_exp_f32 v116, v116\n" \
    "v_exp_f32 v117, v117\n" \
    "v_cvt_pk_bf16_f32 v128, v112, v113\n" \
    "v_cvt_pk_bf16_f32 v129, v114, v115\n" \
    "v_exp_f32 v118, v118\n" \
    "v_exp_f32 v119, v119\n" \
    "v_mfma_f32_32x32x16_bf16 v[96:111], v[140:143], v[84:87], v[96:111]\n" \
    "v_exp_f32 v120, v120\n" \
    "v_exp_f32 v121, v121\n" \
    "v_cvt_pk_bf16_f32 v130, v116, v117\n" \
    "v_cvt_pk_bf16_f32 v131, v118, v119\n" \
    "v_exp_f32 v122, v122\n" \
    "v_exp_f32 v123, v123\n" \
    "v_mfma_f32_32x32x16_bf16 v[0:15], v[172:175], v[132:135], v[0:15]\n" \
    "v_exp_f32 v124, v124\n" \
    "v_exp_f32 v125, v125\n" \
    "v_cvt_pk_bf16_f32 v132, v120, v121\n" \
    "v_cvt_pk_bf16_f32 v133, v122, v123\n" \
    "v_exp_f32 v126, v126\n" \
    "v_exp_f32 v127, v127\n" \
    "v_mfma_f32_32x32x16_bf16 v[16:31], v[168:171], v[128:131], v[16:31]\n" \
    "v_exp_f32 v96, v96\n" \
    "v_exp_f32 v97, v97\n" \
    "v_cvt_pk_bf16_f32 v134, v124, v125\n" \
    "v_cvt_pk_bf16_f32 v135, v126, v127\n" \
    "v_exp_f32 v98, v98\n" \
    "v_exp_f32 v99, v99\n" \
    "v_mfma_f32_32x32x16_bf16 v[112:127], v[136:139], v[88:91], 0\n" \
    "buffer_load_dwordx4 v[136:139], v236, s[40:43], s44 offen offset:0 sc1\n" \
    "v_exp_f32 v100, v100\n" \
    "v_exp_f32 v101, v101\n" \
    "v_cvt_pk_bf16_f32 v128, v96, v97\n" \
    "v_cvt_pk_bf16_f32 v129, v98, v99\n" \
    "v_exp_f32 v102, v102\n" \
    "v_exp_f32 v103, v103\n" \
    "v_mfma_f32_32x32x16_bf16 v[112:127], v[140:143], v[92:95], v[112:127]\n" \
    "buffer_load_dwordx4 v[140:143], v206, s[40:43], s44 offen offset:0 sc1\n" \
    "v_exp_f32 v104, v104\n" \
    "v_exp_f32 v105, v105\n" \
    "v_cvt_pk_bf16_f32 v130, v100, v101\n" \
    "v_cvt_pk_bf16_f32 v131, v102, v103\n" \
    "v_exp_f32 v106, v106\n" \
    "v_exp_f32 v107, v107\n" \
    "v_mfma_f32_32x32x16_bf16 v[16:31], v[172:175], v[132:135], v[16:31]\n" \
    "v_exp_f32 v108, v108\n" \
    "v_exp_f32 v109, v109\n" \
    "v_cvt_pk_bf16_f32 v132, v104, v105\n" \
    "v_cvt_pk_bf16_f32 v133, v106, v107\n" \
    "v_exp_f32 v110, v110\n" \
    "v_exp_f32 v111, v111\n" \
    "v_mfma_f32_32x32x16_bf16 v[32:47], v[168:171], v[128:131], v[32:47]\n" \
    "s_waitcnt vmcnt(12)\n" \
    "v_exp_f32 v112, v112\n" \
    "v_exp_f32 v113, v113\n" \
    "v_cvt_pk_bf16_f32 v134, v108, v109\n" \
    "v_cvt_pk_bf16_f32 v135, v110, v111\n" \
    "v_exp_f32 v114, v114\n" \
    "v_exp_f32 v115, v115\n" \
    "v_mfma_f32_32x32x16_bf16 v[96:111], v[144:147], v[64:67], 0\n" \
    "v_exp_f32 v116, v116\n" \
    "v_exp_f32 v117, v117\n" \
    "v_cvt_pk_bf16_f32 v128, v112, v113\n" \
    "v_cvt_pk_bf16_f32 v129, v114, v115\n" \
    "v_exp_f32 v118, v118\n" \
    "v_exp_f32 v119, v119\n" \
    "v_mfma_f32_32x32x16_bf16 v[96:111], v[148:151], v[68:71], v[96:111]\n" \
    "v_exp_f32 v120, v120\n" \
    "v_exp_f32 v121, v121\n" \
    "v_cvt_pk_bf16_f32 v130, v116, v117\n" \
    "v_cvt_pk_bf16_f32 v131, v118, v119\n" \
    "v_exp_f32 v122, v122\n" \
    "v_exp_f32 v123, v123\n" \
    "v_mfma_f32_32x32x16_bf16 v[32:47], v[172:175], v[132:135], v[32:47]\n" \
    "v_exp_f32 v124, v124\n" \
    "v_exp_f32 v125, v125\n" \
    "v_cvt_pk_bf16_f32 v132, v120, v121\n" \
    "v_cvt_pk_bf16_f32 v133, v122, v123\n" \
    "v_exp_f32 v126, v126\n" \
    "v_exp_f32 v127, v127\n" \
    "v_mfma_f32_32x32x16_bf16 v[48:63], v[168:171], v[128:131], v[48:63]\n" \
    "v_exp_f32 v96, v96\n" \
    "v_exp_f32 v97, v97\n" \
    "v_cvt_pk_bf16_f32 v134, v124, v125\n" \
    "v_cvt_pk_bf16_f32 v135, v126, v127\n" \
    "v_exp_f32 v98, v98\n" \
    "v_exp_f32 v99, v99\n" \
    "v_mfma_f32_32x32x16_bf16 v[112:127], v[144:147], v[72:75], 0\n" \
    "v_exp_f32 v100, v100\n" \
    "v_exp_f32 v101, v101\n" \
    "v_cvt_pk_bf16_f32 v128, v96, v97\n" \
    "v_cvt_pk_bf16_f32 v129, v98, v99\n" \
    "v_exp_f32 v102, v102\n" \
    "v_exp_f32 v103, v103\n" \
    "v_mfma_f32_32x32x16_bf16 v[112:127], v[148:151], v[76:79], v[112:127]\n" \
    "v_exp_f32 v104, v104\n" \
    "v_exp_f32 v105, v105\n" \
    "v_cvt_pk_bf16_f32 v130, v100, v101\n" \
    "v_cvt_pk_bf16_f32 v131, v102, v103\n" \
    "v_exp_f32 v106, v106\n" \
    "v_exp_f32 v107, v107\n" \
    "v_mfma_f32_32x32x16_bf16 v[48:63], v[172:175], v[132:135], v[48:63]\n" \
    "v_exp_f32 v108, v108\n" \
    "v_exp_f32 v109, v109\n" \
    "v_cvt_pk_bf16_f32 v132, v104, v105\n" \
    "v_cvt_pk_bf16_f32 v133, v106, v107\n" \
    "v_exp_f32 v110, v110\n" \
    "v_exp_f32 v111, v111\n" \
    "v_mfma_f32_32x32x16_bf16 v[0:15], v[176:179], v[128:131], v[0:15]\n" \
    "v_exp_f32 v112, v112\n" \
    "v_exp_f32 v113, v113\n" \
    "v_cvt_pk_bf16_f32 v134, v108, v109\n" \
    "v_cvt_pk_bf16_f32 v135, v110, v111\n" \
    "v_exp_f32 v114, v114\n" \
    "v_exp_f32 v115, v115\n" \
    "v_mfma_f32_32x32x16_bf16 v[96:111], v[144:147], v[80:83], 0\n" \
    "v_exp_f32 v116, v116\n" \
    "v_exp_f32 v117, v117\n" \
    "v_cvt_pk_bf16_f32 v128, v112, v113\n" \
    "v_cvt_pk_bf16_f32 v129, v114, v115\n" \
    "v_exp_f32 v118, v118\n" \
    "v_exp_f32 v119, v119\n" \
    "v_mfma_f32_32x32x16_bf16 v[96:111], v[148:151], v[84:87], v[96:111]\n" \
    "v_exp_f32 v120, v120\n" \
    "v_exp_f32 v121, v121\n" \
    "v_cvt_pk_bf16_f32 v130, v116, v117\n" \
    "v_cvt_pk_bf16_f32 v131, v118, v119\n" \
    "v_exp_f32 v122, v122\n" \
    "v_exp_f32 v123, v123\n" \
    "v_mfma_f32_32x32x16_bf16 v[0:15], v[180:183], v[132:135], v[0:15]\n" \
    "v_exp_f32 v124, v124\n" \
    "v_exp_f32 v125, v125\n" \
    "v_cvt_pk_bf16_f32 v132, v120, v121\n" \
    "v_cvt_pk_bf16_f32 v133, v122, v123\n" \
    "v_exp_f32 v126, v126\n" \
    "v_exp_f32 v127, v127\n" \
    "v_mfma_f32_32x32x16_bf16 v[16:31], v[176:179], v[128:131], v[16:31]\n" \
    "v_exp_f32 v96, v96\n" \
    "v_exp_f32 v97, v97\n" \
    "v_cvt_pk_bf16_f32 v134, v124, v125\n" \
    "v_cvt_pk_bf16_f32 v135, v126, v127\n" \
    "v_exp_f32 v98, v98\n" \
    "v_exp_f32 v99, v99\n" \
    "v_mfma_f32_32x32x16_bf16 v[112:127], v[144:147], v[88:91], 0\n" \
    "buffer_load_dwordx4 v[144:147], v236, s[40:43], s44 offen offset:512 sc1\n" \
    "v_exp_f32 v100, v100\n" \
    "v_exp_f32 v101, v101\n" \
    "v_cvt_pk_bf16_f32 v128, v96, v97\n" \
    "v_cvt_pk_bf16_f32 v129, v98, v99\n" \
    "v_exp_f32 v102, v102\n" \
    "v_exp_f32 v103, v103\n" \
    "v_mfma_f32_32x32x16_bf16 v[112:127], v[148:151], v[92:95], v[112:127]\n" \
    "buffer_load_dwordx4 v[148:151], v206, s[40:43], s44 offen offset:512 sc1\n" \
    "v_exp_f32 v104, v104\n" \
    "v_exp_f32 v105, v105\n" \
    "v_cvt_pk_bf16_f32 v130, v100, v101\n" \
    "v_cvt_pk_bf16_f32 v131, v102, v103\n" \
    "v_exp_f32 v106, v106\n" \
    "v_exp_f32 v107, v107\n" \
    "v_mfma_f32_32x32x16_bf16 v[16:31], v[180:183], v[132:135], v[16:31]\n" \
    "v_exp_f32 v108, v108\n" \
    "v_exp_f32 v109, v109\n" \
    "v_cvt_pk_bf16_f32 v132, v104, v105\n" \
    "v_cvt_pk_bf16_f32 v133, v106, v107\n" \
    "v_exp_f32 v110, v110\n" \
    "v_exp_f32 v111, v111\n" \
    "v_mfma_f32_32x32x16_bf16 v[32:47], v[176:179], v[128:131], v[32:47]\n" \
    "s_waitcnt vmcnt(12)\n" \
    "v_exp_f32 v112, v112\n" \
    "v_exp_f32 v113, v113\n" \
    "v_cvt_pk_bf16_f32 v134, v108, v109\n" \
    "v_cvt_pk_bf16_f32 v135, v110, v111\n" \
    "v_exp_f32 v114, v114\n" \
    "v_exp_f32 v115, v115\n" \
    "v_mfma_f32_32x32x16_bf16 v[96:111], v[152:155], v[64:67], 0\n" \
    "v_exp_f32 v116, v116\n" \
    "v_exp_f32 v117, v117\n" \
    "v_cvt_pk_bf16_f32 v128, v112, v113\n" \
    "v_cvt_pk_bf16_f32 v129, v114, v115\n" \
    "v_exp_f32 v118, v118\n" \
    "v_exp_f32 v119, v119\n" \
    "v_mfma_f32_32x32x16_bf16 v[96:111], v[156:159], v[68:71], v[96:111]\n" \
    "v_exp_f32 v120, v120\n" \
    "v_exp_f32 v121, v121\n" \
    "v_cvt_pk_bf16_f32 v130, v116, v117\n" \
    "v_cvt_pk_bf16_f32 v131, v118, v119\n" \
    "v_exp_f32 v122, v122\n" \
    "v_exp_f32 v123, v123\n" \
    "v_mfma_f32_32x32x16_bf16 v[32:47], v[180:183], v[132:135], v[32:47]\n" \
    "v_exp_f32 v124, v124\n" \
    "v_exp_f32 v125, v125\n" \
    "v_cvt_pk_bf16_f32 v132, v120, v121\n" \
    "v_cvt_pk_bf16_f32 v133, v122, v123\n" \
    "v_exp_f32 v126, v126\n" \
    "v_exp_f32 v127, v127\n" \
    "v_mfma_f32_32x32x16_bf16 v[48:63], v[176:179], v[128:131], v[48:63]\n" \
    "v_exp_f32 v96, v96\n" \
    "v_exp_f32 v97, v97\n" \
    "v_cvt_pk_bf16_f32 v134, v124, v125\n" \
    "v_cvt_pk_bf16_f32 v135, v126, v127\n" \
    "v_exp_f32 v98, v98\n" \
    "v_exp_f32 v99, v99\n" \
    "v_mfma_f32_32x32x16_bf16 v[112:127], v[152:155], v[72:75], 0\n" \
    "v_exp_f32 v100, v100\n" \
    "v_exp_f32 v101, v101\n" \
    "v_cvt_pk_bf16_f32 v128, v96, v97\n" \
    "v_cvt_pk_bf16_f32 v129, v98, v99\n" \
    "v_exp_f32 v102, v102\n" \
    "v_exp_f32 v103, v103\n" \
    "v_mfma_f32_32x32x16_bf16 v[112:127], v[156:159], v[76:79], v[112:127]\n" \
    "s_waitcnt vmcnt(6)\n" \
    "v_exp_f32 v104, v104\n" \
    "v_exp_f32 v105, v105\n" \
    "v_cvt_pk_bf16_f32 v130, v100, v101\n" \
    "v_cvt_pk_bf16_f32 v131, v102, v103\n" \
    "v_exp_f32 v106, v106\n" \
    "v_exp_f32 v107, v107\n" \
    "v_mfma_f32_32x32x16_bf16 v[48:63], v[180:183], v[132:135], v[48:63]\n" \
    "s_mov_b64 exec, s[48:49]\n" \
    "buffer_load_dwordx4 v[168:171], v237, s[40:43], s44 offen offset:0 sc1\n" \
    "buffer_load_dwordx4 v[172:175], v207, s[40:43], s44 offen offset:0 sc1\n" \
    "buffer_load_dwordx4 v[176:179], v237, s[40:43], s44 offen offset:512 sc1\n" \
    "buffer_load_dwordx4 v[180:183], v207, s[40:43], s44 offen offset:512 sc1\n" \
    "s_mov_b64 exec, -1\n" \
    "v_exp_f32 v108, v108\n" \
    "v_exp_f32 v109, v109\n" \
    "v_cvt_pk_bf16_f32 v132, v104, v105\n" \
    "v_cvt_pk_bf16_f32 v133, v106, v107\n" \
    "v_exp_f32 v110, v110\n" \
    "v_exp_f32 v111, v111\n" \
    "v_mfma_f32_32x32x16_bf16 v[0:15], v[184:187], v[128:131], v[0:15]\n" \
    "v_exp_f32 v112, v112\n" \
    "v_exp_f32 v113, v113\n" \
    "v_cvt_pk_bf16_f32 v134, v108, v109\n" \
    "v_cvt_pk_bf16_f32 v135, v110, v111\n" \
    "v_exp_f32 v114, v114\n" \
    "v_exp_f32 v115, v115\n" \
    "v_mfma_f32_32x32x16_bf16 v[96:111], v[152:155], v[80:83], 0\n" \
    "v_exp_f32 v116, v116\n" \
    "v_exp_f32 v117, v117\n" \
    "v_cvt_pk_bf16_f32 v128, v112, v113\n" \
    "v_cvt_pk_bf16_f32 v129, v114, v115\n" \
    "v_exp_f32 v118, v118\n" \
    "v_exp_f32 v119, v119\n" \
    "v_mfma_f32_32x32x16_bf16 v[96:111], v[156:159], v[84:87], v[96:111]\n" \
    "v_exp_f32 v120, v120\n" \
    "v_exp_f32 v121, v121\n" \
    "v_cvt_pk_bf16_f32 v130, v116, v117\n" \
    "v_cvt_pk_bf16_f32 v131, v118, v119\n" \
    "v_exp_f32 v122, v122\n" \
    "v_exp_f32 v123, v123\n" \
    "v_mfma_f32_32x32x16_bf16 v[0:15], v[188:191], v[132:135], v[0:15]\n" \
    "v_exp_f32 v124, v124\n" \
    "v_exp_f32 v125, v125\n" \
    "v_cvt_pk_bf16_f32 v132, v120, v121\n" \
    "v_cvt_pk_bf16_f32 v133, v122, v123\n" \
    "v_exp_f32 v126, v126\n" \
    "v_exp_f32 v127, v127\n" \
    "v_mfma_f32_32x32x16_bf16 v[16:31], v[184:187], v[128:131], v[16:31]\n" \
    "v_exp_f32 v96, v96\n" \
    "v_exp_f32 v97, v97\n" \
    "v_cvt_pk_bf16_f32 v134, v124, v125\n" \
    "v_cvt_pk_bf16_f32 v135, v126, v127\n" \
    "v_exp_f32 v98, v98\n" \
    "v_exp_f32 v99, v99\n" \
    "v_mfma_f32_32x32x16_bf16 v[112:127], v[152:155], v[88:91], 0\n" \
    "buffer_load_dwordx4 v[152:155], v236, s[40:43], s44 offen offset:1024 sc1\n" \
    "v_exp_f32 v100, v100\n" \
    "v_exp_f32 v101, v101\n" \
    "v_cvt_pk_bf16_f32 v128, v96, v97\n" \
    "v_cvt_pk_bf16_f32 v129, v98, v99\n" \
    "v_exp_f32 v102, v102\n" \
    "v_exp_f32 v103, v103\n" \
    "v_mfma_f32_32x32x16_bf16 v[112:127], v[156:159], v[92:95], v[112:127]\n" \
    "buffer_load_dwordx4 v[156:159], v206, s[40:43], s44 offen offset:1024 sc1\n" \
    "v_exp_f32 v104, v104\n" \
    "v_exp_f32 v105, v105\n" \
    "v_cvt_pk_bf16_f32 v130, v100, v101\n" \
    "v_cvt_pk_bf16_f32 v131, v102, v103\n" \
    "v_exp_f32 v106, v106\n" \
    "v_exp_f32 v107, v107\n" \
    "v_mfma_f32_32x32x16_bf16 v[16:31], v[188:191], v[132:135], v[16:31]\n" \
    "v_exp_f32 v108, v108\n" \
    "v_exp_f32 v109, v109\n" \
    "v_cvt_pk_bf16_f32 v132, v104, v105\n" \
    "v_cvt_pk_bf16_f32 v133, v106, v107\n" \
    "v_exp_f32 v110, v110\n" \
    "v_exp_f32 v111, v111\n" \
    "v_mfma_f32_32x32x16_bf16 v[32:47], v[184:187], v[128:131], v[32:47]\n" \
    "v_exp_f32 v112, v112\n" \
    "v_exp_f32 v113, v113\n" \
    "v_cvt_pk_bf16_f32 v134, v108, v109\n" \
    "v_cvt_pk_bf16_f32 v135, v110, v111\n" \
    "v_exp_f32 v114, v114\n" \
    "v_exp_f32 v115, v115\n" \
    "v_mfma_f32_32x32x16_bf16 v[96:111], v[160:163], v[64:67], 0\n" \
    "v_exp_f32 v116, v116\n" \
    "v_exp_f32 v117, v117\n" \
    "v_cvt_pk_bf16_f32 v128, v112, v113\n" \
    "v_cvt_pk_bf16_f32 v129, v114, v115\n" \
    "v_exp_f32 v118, v118\n" \
    "v_exp_f32 v119, v119\n" \
    "v_mfma_f32_32x32x16_bf16 v[96:111], v[164:167], v[68:71], v[96:111]\n" \
    "v_exp_f32 v120, v120\n" \
    "v_exp_f32 v121, v121\n" \
    "v_cvt_pk_bf16_f32 v130, v116, v117\n" \
    "v_cvt_pk_bf16_f32 v131, v118, v119\n" \
    "v_exp_f32 v122, v122\n" \
    "v_exp_f32 v123, v123\n" \
    "v_mfma_f32_32x32x16_bf16 v[32:47], v[188:191], v[132:135], v[32:47]\n" \
    "v_exp_f32 v124, v124\n" \
    "v_exp_f32 v125, v125\n" \
    "v_cvt_pk_bf16_f32 v132, v120, v121\n" \
    "v_cvt_pk_bf16_f32 v133, v122, v123\n" \
    "v_exp_f32 v126, v126\n" \
    "v_exp_f32 v127, v127\n" \
    "v_mfma_f32_32x32x16_bf16 v[48:63], v[184:187], v[128:131], v[48:63]\n" \
    "v_exp_f32 v96, v96\n" \
    "v_exp_f32 v97, v97\n" \
    "v_cvt_pk_bf16_f32 v134, v124, v125\n" \
    "v_cvt_pk_bf16_f32 v135, v126, v127\n" \
    "v_exp_f32 v98, v98\n" \
    "v_exp_f32 v99, v99\n" \
    "v_mfma_f32_32x32x16_bf16 v[112:127], v[160:163], v[72:75], 0\n" \
    "v_exp_f32 v100, v100\n" \
    "v_exp_f32 v101, v101\n" \
    "v_cvt_pk_bf16_f32 v128, v96, v97\n" \
    "v_cvt_pk_bf16_f32 v129, v98, v99\n" \
    "v_exp_f32 v102, v102\n" \
    "v_exp_f32 v103, v103\n" \
    "v_mfma_f32_32x32x16_bf16 v[112:127], v[164:167], v[76:79], v[112:127]\n" \
    "s_waitcnt vmcnt(10)\n" \
    "v_exp_f32 v104, v104\n" \
    "v_exp_f32 v105, v105\n" \
    "v_cvt_pk_bf16_f32 v130, v100, v101\n" \
    "v_cvt_pk_bf16_f32 v131, v102, v103\n" \
    "v_exp_f32 v106, v106\n" \
    "v_exp_f32 v107, v107\n" \
    "v_mfma_f32_32x32x16_bf16 v[48:63], v[188:191], v[132:135], v[48:63]\n" \
    "v_exp_f32 v108, v108\n" \
    "v_exp_f32 v109, v109\n" \
    "v_cvt_pk_bf16_f32 v132, v104, v105\n" \
    "v_cvt_pk_bf16_f32 v133, v106, v107\n" \
    "v_exp_f32 v110, v110\n" \
    "v_exp_f32 v111, v111\n" \
    "v_mfma_f32_32x32x16_bf16 v[0:15], v[192:195], v[128:131], v[0:15]\n" \
    "v_exp_f32 v112, v112\n" \
    "v_exp_f32 v113, v113\n" \
    "v_cvt_pk_bf16_f32 v134, v108, v109\n" \
    "v_cvt_pk_bf16_f32 v135, v110, v111\n" \
    "v_exp_f32 v114, v114\n" \
    "v_exp_f32 v115, v115\n" \
    "v_mfma_f32_32x32x16_bf16 v[96:111], v[160:163], v[80:83], 0\n" \
    "v_exp_f32 v116, v116\n" \
    "v_exp_f32 v117, v117\n" \
    "v_cvt_pk_bf16_f32 v128, v112, v113\n" \
    "v_cvt_pk_bf16_f32 v129, v114, v115\n" \
    "v_exp_f32 v118, v118\n" \
    "v_exp_f32 v119, v119\n" \
    "v_mfma_f32_32x32x16_bf16 v[96:111], v[164:167], v[84:87], v[96:111]\n" \
    "v_exp_f32 v120, v120\n" \
    "v_exp_f32 v121, v121\n" \
    "v_cvt_pk_bf16_f32 v130, v116, v117\n" \
    "v_cvt_pk_bf16_f32 v131, v118, v119\n" \
    "v_exp_f32 v122, v122\n" \
    "v_exp_f32 v123, v123\n" \
    "v_mfma_f32_32x32x16_bf16 v[0:15], v[196:199], v[132:135], v[0:15]\n" \
    "v_exp_f32 v124, v124\n" \
    "v_exp_f32 v125, v125\n" \
    "v_cvt_pk_bf16_f32 v132, v120, v121\n" \
    "v_cvt_pk_bf16_f32 v133, v122, v123\n" \
    "v_exp_f32 v126, v126\n" \
    "v_exp_f32 v127, v127\n" \
    "v_mfma_f32_32x32x16_bf16 v[16:31], v[192:195], v[128:131], v[16:31]\n" \
    "v_exp_f32 v96, v96\n" \
    "v_exp_f32 v97, v97\n" \
    "v_cvt_pk_bf16_f32 v134, v124, v125\n" \
    "v_cvt_pk_bf16_f32 v135, v126, v127\n" \
    "v_exp_f32 v98, v98\n" \
    "v_exp_f32 v99, v99\n" \
    "v_mfma_f32_32x32x16_bf16 v[112:127], v[160:163], v[88:91], 0\n" \
    "buffer_load_dwordx4 v[160:163], v236, s[40:43], s44 offen offset:1536 sc1\n" \
    "v_exp_f32 v100, v100\n" \
    "v_exp_f32 v101, v101\n" \
    "v_cvt_pk_bf16_f32 v128, v96, v97\n" \
    "v_cvt_pk_bf16_f32 v129, v98, v99\n" \
    "v_exp_f32 v102, v102\n" \
    "v_exp_f32 v103, v103\n" \
    "v_mfma_f32_32x32x16_bf16 v[112:127], v[164:167], v[92:95], v[112:127]\n" \
    "buffer_load_dwordx4 v[164:167], v206, s[40:43], s44 offen offset:1536 sc1\n" \
    "v_exp_f32 v104, v104\n" \
    "v_exp_f32 v105, v105\n" \
    "v_cvt_pk_bf16_f32 v130, v100, v101\n" \
    "v_cvt_pk_bf16_f32 v131, v102, v103\n" \
    "v_exp_f32 v106, v106\n" \
    "v_exp_f32 v107, v107\n" \
    "v_mfma_f32_32x32x16_bf16 v[16:31], v[196:199], v[132:135], v[16:31]\n" \
    "v_exp_f32 v108, v108\n" \
    "v_exp_f32 v109, v109\n" \
    "v_cvt_pk_bf16_f32 v132, v104, v105\n" \
    "v_cvt_pk_bf16_f32 v133, v106, v107\n" \
    "v_exp_f32 v110, v110\n" \
    "v_exp_f32 v111, v111\n" \
    "v_mfma_f32_32x32x16_bf16 v[32:47], v[192:195], v[128:131], v[32:47]\n" \
    "s_waitcnt vmcnt(10)\n" \
    "v_exp_f32 v112, v112\n" \
    "v_exp_f32 v113, v113\n" \
    "v_cvt_pk_bf16_f32 v134, v108, v109\n" \
    "v_cvt_pk_bf16_f32 v135, v110, v111\n" \
    "v_exp_f32 v114, v114\n" \
    "v_exp_f32 v115, v115\n" \
    "v_mfma_f32_32x32x16_bf16 v[96:111], v[136:139], v[64:67], 0\n" \
    "v_exp_f32 v116, v116\n" \
    "v_exp_f32 v117, v117\n" \
    "v_cvt_pk_bf16_f32 v128, v112, v113\n" \
    "v_cvt_pk_bf16_f32 v129, v114, v115\n" \
    "v_exp_f32 v118, v118\n" \
    "v_exp_f32 v119, v119\n" \
    "v_mfma_f32_32x32x16_bf16 v[96:111], v[140:143], v[68:71], v[96:111]\n" \
    "v_exp_f32 v120, v120\n" \
    "v_exp_f32 v121, v121\n" \
    "v_cvt_pk_bf16_f32 v130, v116, v117\n" \
    "v_cvt_pk_bf16_f32 v131, v118, v119\n" \
    "v_exp_f32 v122, v122\n" \
    "v_exp_f32 v123, v123\n" \
    "v_mfma_f32_32x32x16_bf16 v[32:47], v[196:199], v[132:135], v[32:47]\n" \
    "v_exp_f32 v124, v124\n" \
    "v_exp_f32 v125, v125\n" \
    "v_cvt_pk_bf16_f32 v132, v120, v121\n" \
    "v_cvt_pk_bf16_f32 v133, v122, v123\n" \
    "v_exp_f32 v126, v126\n" \
    "v_exp_f32 v127, v127\n" \
    "v_mfma_f32_32x32x16_bf16 v[48:63], v[192:195], v[128:131], v[48:63]\n" \
    "v_exp_f32 v96, v96\n" \
    "v_exp_f32 v97, v97\n" \
    "v_cvt_pk_bf16_f32 v134, v124, v125\n" \
    "v_cvt_pk_bf16_f32 v135, v126, v127\n" \
    "v_exp_f32 v98, v98\n" \
    "v_exp_f32 v99, v99\n" \
    "v_mfma_f32_32x32x16_bf16 v[112:127], v[136:139], v[72:75], 0\n" \
    "v_exp_f32 v100, v100\n" \
    "v_exp_f32 v101, v101\n" \
    "v_cvt_pk_bf16_f32 v128, v96, v97\n" \
    "v_cvt_pk_bf16_f32 v129, v98, v99\n" \
    "v_exp_f32 v102, v102\n" \
    "v_exp_f32 v103, v103\n" \
    "v_mfma_f32_32x32x16_bf16 v[112:127], v[140:143], v[76:79], v[112:127]\n" \
    "s_waitcnt vmcnt(6)\n" \
    "v_exp_f32 v104, v104\n" \
    "v_exp_f32 v105, v105\n" \
    "v_cvt_pk_bf16_f32 v130, v100, v101\n" \
    "v_cvt_pk_bf16_f32 v131, v102, v103\n" \
    "v_exp_f32 v106, v106\n" \
    "v_exp_f32 v107, v107\n" \
    "v_mfma_f32_32x32x16_bf16 v[48:63], v[196:199], v[132:135], v[48:63]\n" \
    "s_mov_b64 exec, s[48:49]\n" \
    "buffer_load_dwordx4 v[184:187], v237, s[40:43], s44 offen offset:1024 sc1\n" \
    "buffer_load_dwordx4 v[188:191], v207, s[40:43], s44 offen offset:1024 sc1\n" \
    "buffer_load_dwordx4 v[192:195], v237, s[40:43], s44 offen offset:1536 sc1\n" \
    "buffer_load_dwordx4 v[196:199], v207, s[40:43], s44 offen offset:1536 sc1\n" \
    "s_mov_b64 exec, -1\n" \
    "v_exp_f32 v108, v108\n" \
    "v_exp_f32 v109, v109\n" \
    "v_cvt_pk_bf16_f32 v132, v104, v105\n" \
    "v_cvt_pk_bf16_f32 v133, v106, v107\n" \
    "v_exp_f32 v110, v110\n" \
    "v_exp_f32 v111, v111\n" \
    "v_mfma_f32_32x32x16_bf16 v[0:15], v[168:171], v[128:131], v[0:15]\n" \
    "v_exp_f32 v112, v112\n" \
    "v_exp_f32 v113, v113\n" \
    "v_cvt_pk_bf16_f32 v134, v108, v109\n" \
    "v_cvt_pk_bf16_f32 v135, v110, v111\n" \
    "v_exp_f32 v114, v114\n" \
    "v_exp_f32 v115, v115\n" \
    "v_mfma_f32_32x32x16_bf16 v[96:111], v[136:139], v[80:83], 0\n" \
    "s_mov_b64 exec, 1\n" \
    "buffer_store_dwordx2 %[tag], v205, s[40:43], 0 offen sc1\n" \
    "s_mov_b64 exec, -1\n" \
    "v_exp_f32 v116, v116\n" \
    "v_exp_f32 v117, v117\n" \
    "v_cvt_pk_bf16_f32 v128, v112, v113\n" \
    "v_cvt_pk_bf16_f32 v129, v114, v115\n" \
    "v_exp_f32 v118, v118\n" \
    "v_exp_f32 v119, v119\n" \
    "v_mfma_f32_32x32x16_bf16 v[96:111], v[140:143], v[84:87], v[96:111]\n" \
    "v_exp_f32 v120, v120\n" \
    "v_exp_f32 v121, v121\n" \
    "v_cvt_pk_bf16_f32 v130, v116, v117\n" \
    "v_cvt_pk_bf16_f32 v131, v118, v119\n" \
    "v_exp_f32 v122, v122\n" \
    "v_exp_f32 v123, v123\n" \
    "v_mfma_f32_32x32x16_bf16 v[0:15], v[172:175], v[132:135], v[0:15]\n" \
    "v_exp_f32 v124, v124\n" \
    "v_exp_f32 v125, v125\n" \
    "v_cvt_pk_bf16_f32 v132, v120, v121\n" \
    "v_cvt_pk_bf16_f32 v133, v122, v123\n" \
    "v_exp_f32 v126, v126\n" \
    "v_exp_f32 v127, v127\n" \
    "v_mfma_f32_32x32x16_bf16 v[16:31], v[168:171], v[128:131], v[16:31]\n" \
    "v_exp_f32 v96, v96\n" \
    "v_exp_f32 v97, v97\n" \
    "v_cvt_pk_bf16_f32 v134, v124, v125\n" \
    "v_cvt_pk_bf16_f32 v135, v126, v127\n" \
    "v_exp_f32 v98, v98\n" \
    "v_exp_f32 v99, v99\n" \
    "v_mfma_f32_32x32x16_bf16 v[112:127], v[136:139], v[88:91], 0\n" \
    "buffer_load_dwordx4 v[136:139], v236, s[40:43], s44 offen offset:2048 sc1\n" \
    "v_exp_f32 v100, v100\n" \
    "v_exp_f32 v101, v101\n" \
    "v_cvt_pk_bf16_f32 v128, v96, v97\n" \
    "v_cvt_pk_bf16_f32 v129, v98, v99\n" \
    "v_exp_f32 v102, v102\n" \
    "v_exp_f32 v103, v103\n" \
    "v_mfma_f32_32x32x16_bf16 v[112:127], v[140:143], v[92:95], v[112:127]\n" \
    "buffer_load_dwordx4 v[140:143], v206, s[40:43], s44 offen offset:2048 sc1\n" \
    "v_exp_f32 v104, v104\n" \
    "v_exp_f32 v105, v105\n" \
    "v_cvt_pk_bf16_f32 v130, v100, v101\n" \
    "v_cvt_pk_bf16_f32 v131, v102, v103\n" \
    "v_exp_f32 v106, v106\n" \
    "v_exp_f32 v107, v107\n" \
    "v_mfma_f32_32x32x16_bf16 v[16:31], v[172:175], v[132:135], v[16:31]\n" \
    "v_exp_f32 v108, v108\n" \
    "v_exp_f32 v109, v109\n" \
    "v_cvt_pk_bf16_f32 v132, v104, v105\n" \
    "v_cvt_pk_bf16_f32 v133, v106, v107\n" \
    "v_exp_f32 v110, v110\n" \
    "v_exp_f32 v111, v111\n" \
    "v_mfma_f32_32x32x16_bf16 v[32:47], v[168:171], v[128:131], v[32:47]\n" \
    "v_exp_f32 v112, v112\n" \
    "v_exp_f32 v113, v113\n" \
    "v_cvt_pk_bf16_f32 v134, v108, v109\n" \
    "v_cvt_pk_bf16_f32 v135, v110, v111\n" \
    "v_exp_f32 v114, v114\n" \
    "v_exp_f32 v115, v115\n" \
    "v_mfma_f32_32x32x16_bf16 v[96:111], v[144:147], v[64:67], 0\n" \
    "v_exp_f32 v116, v116\n" \
    "v_exp_f32 v117, v117\n" \
    "v_cvt_pk_bf16_f32 v128, v112, v113\n" \
    "v_cvt_pk_bf16_f32 v129, v114, v115\n" \
    "v_exp_f32 v118, v118\n" \
    "v_exp_f32 v119, v119\n" \
    "v_mfma_f32_32x32x16_bf16 v[96:111], v[148:151], v[68:71], v[96:111]\n" \
    "v_exp_f32 v120, v120\n" \
    "v_exp_f32 v121, v121\n" \
    "v_cvt_pk_bf16_f32 v130, v116, v117\n" \
    "v_cvt_pk_bf16_f32 v131, v118, v119\n" \
    "v_exp_f32 v122, v122\n" \
    "v_exp_f32 v123, v123\n" \
    "v_mfma_f32_32x32x16_bf16 v[32:47], v[172:175], v[132:135], v[32:47]\n" \
    "v_exp_f32 v124, v124\n" \
    "v_exp_f32 v125, v125\n" \
    "v_cvt_pk_bf16_f32 v132, v120, v121\n" \
    "v_cvt_pk_bf16_f32 v133, v122, v123\n" \
    "v_exp_f32 v126, v126\n" \
    "v_exp_f32 v127, v127\n" \
    "v_mfma_f32_32x32x16_bf16 v[48:63], v[168:171], v[128:131], v[48:63]\n" \
    "v_exp_f32 v96, v96\n" \
    "v_exp_f32 v97, v97\n" \
    "v_cvt_pk_bf16_f32 v134, v124, v125\n" \
    "v_cvt_pk_bf16_f32 v135, v126, v127\n" \
    "v_exp_f32 v98, v98\n" \
    "v_exp_f32 v99, v99\n" \
    "v_mfma_f32_32x32x16_bf16 v[112:127], v[144:147], v[72:75], 0\n" \
    "v_exp_f32 v100, v100\n" \
    "v_exp_f32 v101, v101\n" \
    "v_cvt_pk_bf16_f32 v128, v96, v97\n" \
    "v_cvt_pk_bf16_f32 v129, v98, v99\n" \
    "v_exp_f32 v102, v102\n" \
    "v_exp_f32 v103, v103\n" \
    "v_mfma_f32_32x32x16_bf16 v[112:127], v[148:151], v[76:79], v[112:127]\n" \
    "s_waitcnt vmcnt(11)\n" \
    "v_exp_f32 v104, v104\n" \
    "v_exp_f32 v105, v105\n" \
    "v_cvt_pk_bf16_f32 v130, v100, v101\n" \
    "v_cvt_pk_bf16_f32 v131, v102, v103\n" \
    "v_exp_f32 v106, v106\n" \
    "v_exp_f32 v107, v107\n" \
    "v_mfma_f32_32x32x16_bf16 v[48:63], v[172:175], v[132:135], v[48:63]\n" \
    "v_exp_f32 v108, v108\n" \
    "v_exp_f32 v109, v109\n" \
    "v_cvt_pk_bf16_f32 v132, v104, v105\n" \
    "v_cvt_pk_bf16_f32 v133, v106, v107\n" \
    "v_exp_f32 v110, v110\n" \
    "v_exp_f32 v111, v111\n" \
    "v_mfma_f32_32x32x16_bf16 v[0:15], v[176:179], v[128:131], v[0:15]\n" \
    "v_exp_f32 v112, v112\n" \
    "v_exp_f32 v113, v113\n" \
    "v_cvt_pk_bf16_f32 v134, v108, v109\n" \
    "v_cvt_pk_bf16_f32 v135, v110, v111\n" \
    "v_exp_f32 v114, v114\n" \
    "v_exp_f32 v115, v115\n" \
    "v_mfma_f32_32x32x16_bf16 v[96:111], v[144:147], v[80:83], 0\n" \
    "v_exp_f32 v116, v116\n" \
    "v_exp_f32 v117, v117\n" \
    "v_cvt_pk_bf16_f32 v128, v112, v113\n" \
    "v_cvt_pk_bf16_f32 v129, v114, v115\n" \
    "v_exp_f32 v118, v118\n" \
    "v_exp_f32 v119, v119\n" \
    "v_mfma_f32_32x32x16_bf16 v[96:111], v[148:151], v[84:87], v[96:111]\n" \
    "v_exp_f32 v120, v120\n" \
    "v_exp_f32 v121, v121\n" \
    "v_cvt_pk_bf16_f32 v130, v116, v117\n" \
    "v_cvt_pk_bf16_f32 v131, v118, v119\n" \
    "v_exp_f32 v122, v122\n" \
    "v_exp_f32 v123, v123\n" \
    "v_mfma_f32_32x32x16_bf16 v[0:15], v[180:183], v[132:135], v[0:15]\n" \
    "v_exp_f32 v124, v124\n" \
    "v_exp_f32 v125, v125\n" \
    "v_cvt_pk_bf16_f32 v132, v120, v121\n" \
    "v_cvt_pk_bf16_f32 v133, v122, v123\n" \
    "v_exp_f32 v126, v126\n" \
    "v_exp_f32 v127, v127\n" \
    "v_mfma_f32_32x32x16_bf16 v[16:31], v[176:179], v[128:131], v[16:31]\n" \
    "v_exp_f32 v96, v96\n" \
    "v_exp_f32 v97, v97\n" \
    "v_cvt_pk_bf16_f32 v134, v124, v125\n" \
    "v_cvt_pk_bf16_f32 v135, v126, v127\n" \
    "v_exp_f32 v98, v98\n" \
    "v_exp_f32 v99, v99\n" \
    "v_mfma_f32_32x32x16_bf16 v[112:127], v[144:147], v[88:91], 0\n" \
    "buffer_load_dwordx4 v[144:147], v236, s[40:43], s44 offen offset:2560 sc1\n" \
    "v_exp_f32 v100, v100\n" \
    "v_exp_f32 v101, v101\n" \
    "v_cvt_pk_bf16_f32 v128, v96, v97\n" \
    "v_cvt_pk_bf16_f32 v129, v98, v99\n" \
    "v_exp_f32 v102, v102\n" \
    "v_exp_f32 v103, v103\n" \
    "v_mfma_f32_32x32x16_bf16 v[112:127], v[148:151], v[92:95], v[112:127]\n" \
    "buffer_load_dwordx4 v[148:151], v206, s[40:43], s44 offen offset:2560 sc1\n" \
    "v_exp_f32 v104, v104\n" \
    "v_exp_f32 v105, v105\n" \
    "v_cvt_pk_bf16_f32 v130, v100, v101\n" \
    "v_cvt_pk_bf16_f32 v131, v102, v103\n" \
    "v_exp_f32 v106, v106\n" \
    "v_exp_f32 v107, v107\n" \
    "v_mfma_f32_32x32x16_bf16 v[16:31], v[180:183], v[132:135], v[16:31]\n" \
    "v_exp_f32 v108, v108\n" \
    "v_exp_f32 v109, v109\n" \
    "v_cvt_pk_bf16_f32 v132, v104, v105\n" \
    "v_cvt_pk_bf16_f32 v133, v106, v107\n" \
    "v_exp_f32 v110, v110\n" \
    "v_exp_f32 v111, v111\n" \
    "v_mfma_f32_32x32x16_bf16 v[32:47], v[176:179], v[128:131], v[32:47]\n" \
    "s_waitcnt vmcnt(11)\n" \
    "v_exp_f32 v112, v112\n" \
    "v_exp_f32 v113, v113\n" \
    "v_cvt_pk_bf16_f32 v134, v108, v109\n" \
    "v_cvt_pk_bf16_f32 v135, v110, v111\n" \
    "v_exp_f32 v114, v114\n" \
    "v_exp_f32 v115, v115\n" \
    "v_mfma_f32_32x32x16_bf16 v[96:111], v[152:155], v[64:67], 0\n" \
    "v_exp_f32 v116, v116\n" \
    "v_exp_f32 v117, v117\n" \
    "v_cvt_pk_bf16_f32 v128, v112, v113\n" \
    "v_cvt_pk_bf16_f32 v129, v114, v115\n" \
    "v_exp_f32 v118, v118\n" \
    "v_exp_f32 v119, v119\n" \
    "v_mfma_f32_32x32x16_bf16 v[96:111], v[156:159], v[68:71], v[96:111]\n" \
    "v_exp_f32 v120, v120\n" \
    "v_exp_f32 v121, v121\n" \
    "v_cvt_pk_bf16_f32 v130, v116, v117\n" \
    "v_cvt_pk_bf16_f32 v131, v118, v119\n" \
    "v_exp_f32 v122, v122\n" \
    "v_exp_f32 v123, v123\n" \
    "v_mfma_f32_32x32x16_bf16 v[32:47], v[180:183], v[132:135], v[32:47]\n" \
    "v_exp_f32 v124, v124\n" \
    "v_exp_f32 v125, v125\n" \
    "v_cvt_pk_bf16_f32 v132, v120, v121\n" \
    "v_cvt_pk_bf16_f32 v133, v122, v123\n" \
    "v_exp_f32 v126, v126\n" \
    "v_exp_f32 v127, v127\n" \
    "v_mfma_f32_32x32x16_bf16 v[48:63], v[176:179], v[128:131], v[48:63]\n" \
    "v_exp_f32 v96, v96\n" \
    "v_exp_f32 v97, v97\n" \
    "v_cvt_pk_bf16_f32 v134, v124, v125\n" \
    "v_cvt_pk_bf16_f32 v135, v126, v127\n" \
    "v_exp_f32 v98, v98\n" \
    "v_exp_f32 v99, v99\n" \
    "v_mfma_f32_32x32x16_bf16 v[112:127], v[152:155], v[72:75], 0\n" \
    "v_exp_f32 v100, v100\n" \
    "v_exp_f32 v101, v101\n" \
    "v_cvt_pk_bf16_f32 v128, v96, v97\n" \
    "v_cvt_pk_bf16_f32 v129, v98, v99\n" \
    "v_exp_f32 v102, v102\n" \
    "v_exp_f32 v103, v103\n" \
    "v_mfma_f32_32x32x16_bf16 v[112:127], v[156:159], v[76:79], v[112:127]\n" \
    "s_waitcnt vmcnt(7)\n" \
    "v_exp_f32 v104, v104\n" \
    "v_exp_f32 v105, v105\n" \
    "v_cvt_pk_bf16_f32 v130, v100, v101\n" \
    "v_cvt_pk_bf16_f32 v131, v102, v103\n" \
    "v_exp_f32 v106, v106\n" \
    "v_exp_f32 v107, v107\n" \
    "v_mfma_f32_32x32x16_bf16 v[48:63], v[180:183], v[132:135], v[48:63]\n" \
    "s_mov_b64 exec, s[48:49]\n" \
    "buffer_load_dwordx4 v[168:171], v237, s[40:43], s44 offen offset:2048 sc1\n" \
    "buffer_load_dwordx4 v[172:175], v207, s[40:43], s44 offen offset:2048 sc1\n" \
    "buffer_load_dwordx4 v[176:179], v237, s[40:43], s44 offen offset:2560 sc1\n" \
    "buffer_load_dwordx4 v[180:183], v207, s[40:43], s44 offen offset:2560 sc1\n" \
    "s_mov_b64 exec, -1\n" \
    "v_exp_f32 v108, v108\n" \
    "v_exp_f32 v109, v109\n" \
    "v_cvt_pk_bf16_f32 v132, v104, v105\n" \
    "v_cvt_pk_bf16_f32 v133, v106, v107\n" \
    "v_exp_f32 v110, v110\n" \
    "v_exp_f32 v111, v111\n" \
    "v_mfma_f32_32x32x16_bf16 v[0:15], v[184:187], v[128:131], v[0:15]\n" \
    "v_exp_f32 v112, v112\n" \
    "v_exp_f32 v113, v113\n" \
    "v_cvt_pk_bf16_f32 v134, v108, v109\n" \
    "v_cvt_pk_bf16_f32 v135, v110, v111\n" \
    "v_exp_f32 v114, v114\n" \
    "v_exp_f32 v115, v115\n" \
    "v_mfma_f32_32x32x16_bf16 v[96:111], v[152:155], v[80:83], 0\n" \
    "v_exp_f32 v116, v116\n" \
    "v_exp_f32 v117, v117\n" \
    "v_cvt_pk_bf16_f32 v128, v112, v113\n" \
    "v_cvt_pk_bf16_f32 v129, v114, v115\n" \
    "v_exp_f32 v118, v118\n" \
    "v_exp_f32 v119, v119\n" \
    "v_mfma_f32_32x32x16_bf16 v[96:111], v[156:159], v[84:87], v[96:111]\n" \
    "v_exp_f32 v120, v120\n" \
    "v_exp_f32 v121, v121\n" \
    "v_cvt_pk_bf16_f32 v130, v116, v117\n" \
    "v_cvt_pk_bf16_f32 v131, v118, v119\n" \
    "v_exp_f32 v122, v122\n" \
    "v_exp_f32 v123, v123\n" \
    "v_mfma_f32_32x32x16_bf16 v[0:15], v[188:191], v[132:135], v[0:15]\n" \
    "v_exp_f32 v124, v124\n" \
    "v_exp_f32 v125, v125\n" \
    "v_cvt_pk_bf16_f32 v132, v120, v121\n" \
    "v_cvt_pk_bf16_f32 v133, v122, v123\n" \
    "v_exp_f32 v126, v126\n" \
    "v_exp_f32 v127, v127\n" \
    "v_mfma_f32_32x32x16_bf16 v[16:31], v[184:187], v[128:131], v[16:31]\n" \
    "v_exp_f32 v96, v96\n" \
    "v_exp_f32 v97, v97\n" \
    "v_cvt_pk_bf16_f32 v134, v124, v125\n" \
    "v_cvt_pk_bf16_f32 v135, v126, v127\n" \
    "v_exp_f32 v98, v98\n" \
    "v_exp_f32 v99, v99\n" \
    "v_mfma_f32_32x32x16_bf16 v[112:127], v[152:155], v[88:91], 0\n" \
    "buffer_load_dwordx4 v[152:155], v236, s[40:43], s44 offen offset:3072 sc1\n" \
    "v_exp_f32 v100, v100\n" \
    "v_exp_f32 v101, v101\n" \
    "v_cvt_pk_bf16_f32 v128, v96, v97\n" \
    "v_cvt_pk_bf16_f32 v129, v98, v99\n" \
    "v_exp_f32 v102, v102\n" \
    "v_exp_f32 v103, v103\n" \
    "v_mfma_f32_32x32x16_bf16 v[112:127], v[156:159], v[92:95], v[112:127]\n" \
    "buffer_load_dwordx4 v[156:159], v206, s[40:43], s44 offen offset:3072 sc1\n" \
    "v_exp_f32 v104, v104\n" \
    "v_exp_f32 v105, v105\n" \
    "v_cvt_pk_bf16_f32 v130, v100, v101\n" \
    "v_cvt_pk_bf16_f32 v131, v102, v103\n" \
    "v_exp_f32 v106, v106\n" \
    "v_exp_f32 v107, v107\n" \
    "v_mfma_f32_32x32x16_bf16 v[16:31], v[188:191], v[132:135], v[16:31]\n" \
    "v_exp_f32 v108, v108\n" \
    "v_exp_f32 v109, v109\n" \
    "v_cvt_pk_bf16_f32 v132, v104, v105\n" \
    "v_cvt_pk_bf16_f32 v133, v106, v107\n" \
    "v_exp_f32 v110, v110\n" \
    "v_exp_f32 v111, v111\n" \
    "v_mfma_f32_32x32x16_bf16 v[32:47], v[184:187], v[128:131], v[32:47]\n" \
    "v_exp_f32 v112, v112\n" \
    "v_exp_f32 v113, v113\n" \
    "v_cvt_pk_bf16_f32 v134, v108, v109\n" \
    "v_cvt_pk_bf16_f32 v135, v110, v111\n" \
    "v_exp_f32 v114, v114\n" \
    "v_exp_f32 v115, v115\n" \
    "v_mfma_f32_32x32x16_bf16 v[96:111], v[160:163], v[64:67], 0\n" \
    "v_exp_f32 v116, v116\n" \
    "v_exp_f32 v117, v117\n" \
    "v_cvt_pk_bf16_f32 v128, v112, v113\n" \
    "v_cvt_pk_bf16_f32 v129, v114, v115\n" \
    "v_exp_f32 v118, v118\n" \
    "v_exp_f32 v119, v119\n" \
    "v_mfma_f32_32x32x16_bf16 v[96:111], v[164:167], v[68:71], v[96:111]\n" \
    "v_exp_f32 v120, v120\n" \
    "v_exp_f32 v121, v121\n" \
    "v_cvt_pk_bf16_f32 v130, v116, v117\n" \
    "v_cvt_pk_bf16_f32 v131, v118, v119\n" \
    "v_exp_f32 v122, v122\n" \
    "v_exp_f32 v123, v123\n" \
    "v_mfma_f32_32x32x16_bf16 v[32:47], v[188:191], v[132:135], v[32:47]\n" \
    "v_exp_f32 v124, v124\n" \
    "v_exp_f32 v125, v125\n" \
    "v_cvt_pk_bf16_f32 v132, v120, v121\n" \
    "v_cvt_pk_bf16_f32 v133, v122, v123\n" \
    "v_exp_f32 v126, v126\n" \
    "v_exp_f32 v127, v127\n" \
    "v_mfma_f32_32x32x16_bf16 v[48:63], v[184:187], v[128:131], v[48:63]\n" \
    "v_exp_f32 v96, v96\n" \
    "v_exp_f32 v97, v97\n" \
    "v_cvt_pk_bf16_f32 v134, v124, v125\n" \
    "v_cvt_pk_bf16_f32 v135, v126, v127\n" \
    "v_exp_f32 v98, v98\n" \
    "v_exp_f32 v99, v99\n" \
    "v_mfma_f32_32x32x16_bf16 v[112:127], v[160:163], v[72:75], 0\n" \
    "v_exp_f32 v100, v100\n" \
    "v_exp_f32 v101, v101\n" \
    "v_cvt_pk_bf16_f32 v128, v96, v97\n" \
    "v_cvt_pk_bf16_f32 v129, v98, v99\n" \
    "v_exp_f32 v102, v102\n" \
    "v_exp_f32 v103, v103\n" \
    "v_mfma_f32_32x32x16_bf16 v[112:127], v[164:167], v[76:79], v[112:127]\n" \
    "s_waitcnt vmcnt(11)\n" \
    "v_exp_f32 v104, v104\n" \
    "v_exp_f32 v105, v105\n" \
    "v_cvt_pk_bf16_f32 v130, v100, v101\n" \
    "v_cvt_pk_bf16_f32 v131, v102, v103\n" \
    "v_exp_f32 v106, v106\n" \
    "v_exp_f32 v107, v107\n" \
    "v_mfma_f32_32x32x16_bf16 v[48:63], v[188:191], v[132:135], v[48:63]\n" \
    "v_exp_f32 v108, v108\n" \
    "v_exp_f32 v109, v109\n" \
    "v_cvt_pk_bf16_f32 v132, v104, v105\n" \
    "v_cvt_pk_bf16_f32 v133, v106, v107\n" \
    "v_exp_f32 v110, v110\n" \
    "v_exp_f32 v111, v111\n" \
    "v_mfma_f32_32x32x16_bf16 v[0:15], v[192:195], v[128:131], v[0:15]\n" \
    "v_exp_f32 v112, v112\n" \
    "v_exp_f32 v113, v113\n" \
    "v_cvt_pk_bf16_f32 v134, v108, v109\n" \
    "v_cvt_pk_bf16_f32 v135, v110, v111\n" \
    "v_exp_f32 v114, v114\n" \
    "v_exp_f32 v115, v115\n" \
    "v_mfma_f32_32x32x16_bf16 v[96:111], v[160:163], v[80:83], 0\n" \
    "v_exp_f32 v116, v116\n" \
    "v_exp_f32 v117, v117\n" \
    "v_cvt_pk_bf16_f32 v128, v112, v113\n" \
    "v_cvt_pk_bf16_f32 v129, v114, v115\n" \
    "v_exp_f32 v118, v118\n" \
    "v_exp_f32 v119, v119\n" \
    "v_mfma_f32_32x32x16_bf16 v[96:111], v[164:167], v[84:87], v[96:111]\n" \
    "v_exp_f32 v120, v120\n" \
    "v_exp_f32 v121, v121\n" \
    "v_cvt_pk_bf16_f32 v130, v116, v117\n" \
    "v_cvt_pk_bf16_f32 v131, v118, v119\n" \
    "v_exp_f32 v122, v122\n" \
    "v_exp_f32 v123, v123\n" \
    "v_mfma_f32_32x32x16_bf16 v[0:15], v[196:199], v[132:135], v[0:15]\n" \
    "v_exp_f32 v124, v124\n" \
    "v_exp_f32 v125, v125\n" \
    "v_cvt_pk_bf16_f32 v132, v120, v121\n" \
    "v_cvt_pk_bf16_f32 v133, v122, v123\n" \
    "v_exp_f32 v126, v126\n" \
    "v_exp_f32 v127, v127\n" \
    "v_mfma_f32_32x32x16_bf16 v[16:31], v[192:195], v[128:131], v[16:31]\n" \
    "v_exp_f32 v96, v96\n" \
    "v_exp_f32 v97, v97\n" \
    "v_cvt_pk_bf16_f32 v134, v124, v125\n" \
    "v_cvt_pk_bf16_f32 v135, v126, v127\n" \
    "v_exp_f32 v98, v98\n" \
    "v_exp_f32 v99, v99\n" \
    "v_mfma_f32_32x32x16_bf16 v[112:127], v[160:163], v[88:91], 0\n" \
    "buffer_load_dwordx4 v[160:163], v236, s[40:43], s44 offen offset:3584 sc1\n" \
    "v_exp_f32 v100, v100\n" \
    "v_exp_f32 v101, v101\n" \
    "v_cvt_pk_bf16_f32 v128, v96, v97\n" \
    "v_cvt_pk_bf16_f32 v129, v98, v99\n" \
    "v_exp_f32 v102, v102\n" \
    "v_exp_f32 v103, v103\n" \
    "v_mfma_f32_32x32x16_bf16 v[112:127], v[164:167], v[92:95], v[112:127]\n" \
    "buffer_load_dwordx4 v[164:167], v206, s[40:43], s44 offen offset:3584 sc1\n" \
    "v_exp_f32 v104, v104\n" \
    "v_exp_f32 v105, v105\n" \
    "v_cvt_pk_bf16_f32 v130, v100, v101\n" \
    "v_cvt_pk_bf16_f32 v131, v102, v103\n" \
    "v_exp_f32 v106, v106\n" \
    "v_exp_f32 v107, v107\n" \
    "v_mfma_f32_32x32x16_bf16 v[16:31], v[196:199], v[132:135], v[16:31]\n" \
    "v_exp_f32 v108, v108\n" \
    "v_exp_f32 v109, v109\n" \
    "v_cvt_pk_bf16_f32 v132, v104, v105\n" \
    "v_cvt_pk_bf16_f32 v133, v106, v107\n" \
    "v_exp_f32 v110, v110\n" \
    "v_exp_f32 v111, v111\n" \
    "v_mfma_f32_32x32x16_bf16 v[32:47], v[192:195], v[128:131], v[32:47]\n" \
    "s_waitcnt vmcnt(10)\n" \
    "v_exp_f32 v112, v112\n" \
    "v_exp_f32 v113, v113\n" \
    "v_cvt_pk_bf16_f32 v134, v108, v109\n" \
    "v_cvt_pk_bf16_f32 v135, v110, v111\n" \
    "v_exp_f32 v114, v114\n" \
    "v_exp_f32 v115, v115\n" \
    "v_mfma_f32_32x32x16_bf16 v[96:111], v[136:139], v[64:67], 0\n" \
    "v_exp_f32 v116, v116\n" \
    "v_exp_f32 v117, v117\n" \
    "v_cvt_pk_bf16_f32 v128, v112, v113\n" \
    "v_cvt_pk_bf16_f32 v129, v114, v115\n" \
    "v_exp_f32 v118, v118\n" \
    "v_exp_f32 v119, v119\n" \
    "v_mfma_f32_32x32x16_bf16 v[96:111], v[140:143], v[68:71], v[96:111]\n" \
    "v_exp_f32 v120, v120\n" \
    "v_exp_f32 v121, v121\n" \
    "v_cvt_pk_bf16_f32 v130, v116, v117\n" \
    "v_cvt_pk_bf16_f32 v131, v118, v119\n" \
    "v_exp_f32 v122, v122\n" \
    "v_exp_f32 v123, v123\n" \
    "v_mfma_f32_32x32x16_bf16 v[32:47], v[196:199], v[132:135], v[32:47]\n" \
    "v_exp_f32 v124, v124\n" \
    "v_exp_f32 v125, v125\n" \
    "v_cvt_pk_bf16_f32 v132, v120, v121\n" \
    "v_cvt_pk_bf16_f32 v133, v122, v123\n" \
    "v_exp_f32 v126, v126\n" \
    "v_exp_f32 v127, v127\n" \
    "v_mfma_f32_32x32x16_bf16 v[48:63], v[192:195], v[128:131], v[48:63]\n" \
    "v_exp_f32 v96, v96\n" \
    "v_exp_f32 v97, v97\n" \
    "v_cvt_pk_bf16_f32 v134, v124, v125\n" \
    "v_cvt_pk_bf16_f32 v135, v126, v127\n" \
    "v_exp_f32 v98, v98\n" \
    "v_exp_f32 v99, v99\n" \
    "v_mfma_f32_32x32x16_bf16 v[112:127], v[136:139], v[72:75], 0\n" \
    "v_exp_f32 v100, v100\n" \
    "v_exp_f32 v101, v101\n" \
    "v_cvt_pk_bf16_f32 v128, v96, v97\n" \
    "v_cvt_pk_bf16_f32 v129, v98, v99\n" \
    "v_exp_f32 v102, v102\n" \
    "v_exp_f32 v103, v103\n" \
    "v_mfma_f32_32x32x16_bf16 v[112:127], v[140:143], v[76:79], v[112:127]\n" \
    "s_waitcnt vmcnt(6)\n" \
    "v_exp_f32 v104, v104\n" \
    "v_exp_f32 v105, v105\n" \
    "v_cvt_pk_bf16_f32 v130, v100, v101\n" \
    "v_cvt_pk_bf16_f32 v131, v102, v103\n" \
    "v_exp_f32 v106, v106\n" \
    "v_exp_f32 v107, v107\n" \
    "v_mfma_f32_32x32x16_bf16 v[48:63], v[196:199], v[132:135], v[48:63]\n" \
    "s_mov_b64 exec, s[48:49]\n" \
    "buffer_load_dwordx4 v[184:187], v237, s[40:43], s44 offen offset:3072 sc1\n" \
    "buffer_load_dwordx4 v[188:191], v207, s[40:43], s44 offen offset:3072 sc1\n" \
    "buffer_load_dwordx4 v[192:195], v237, s[40:43], s44 offen offset:3584 sc1\n" \
    "buffer_load_dwordx4 v[196:199], v207, s[40:43], s44 offen offset:3584 sc1\n" \
    "s_mov_b64 exec, -1\n" \
    "v_exp_f32 v108, v108\n" \
    "v_exp_f32 v109, v109\n" \
    "v_cvt_pk_bf16_f32 v132, v104, v105\n" \
    "v_cvt_pk_bf16_f32 v133, v106, v107\n" \
    "v_exp_f32 v110, v110\n" \
    "v_exp_f32 v111, v111\n" \
    "v_mfma_f32_32x32x16_bf16 v[0:15], v[168:171], v[128:131], v[0:15]\n" \
    "v_exp_f32 v112, v112\n" \
    "v_exp_f32 v113, v113\n" \
    "v_cvt_pk_bf16_f32 v134, v108, v109\n" \
    "v_cvt_pk_bf16_f32 v135, v110, v111\n" \
    "v_exp_f32 v114, v114\n" \
    "v_exp_f32 v115, v115\n" \
    "v_mfma_f32_32x32x16_bf16 v[96:111], v[136:139], v[80:83], 0\n" \
    "v_exp_f32 v116, v116\n" \
    "v_exp_f32 v117, v117\n" \
    "v_cvt_pk_bf16_f32 v128, v112, v113\n" \
    "v_cvt_pk_bf16_f32 v129, v114, v115\n" \
    "v_exp_f32 v118, v118\n" \
    "v_exp_f32 v119, v119\n" \
    "v_mfma_f32_32x32x16_bf16 v[96:111], v[140:143], v[84:87], v[96:111]\n" \
    "v_exp_f32 v120, v120\n" \
    "v_exp_f32 v121, v121\n" \
    "v_cvt_pk_bf16_f32 v130, v116, v117\n" \
    "v_cvt_pk_bf16_f32 v131, v118, v119\n" \
    "v_exp_f32 v122, v122\n" \
    "v_exp_f32 v123, v123\n" \
    "v_mfma_f32_32x32x16_bf16 v[0:15], v[172:175], v[132:135], v[0:15]\n" \
    "v_exp_f32 v124, v124\n" \
    "v_exp_f32 v125, v125\n" \
    "v_cvt_pk_bf16_f32 v132, v120, v121\n" \
    "v_cvt_pk_bf16_f32 v133, v122, v123\n" \
    "v_exp_f32 v126, v126\n" \
    "v_exp_f32 v127, v127\n" \
    "v_mfma_f32_32x32x16_bf16 v[16:31], v[168:171], v[128:131], v[16:31]\n" \
    "v_exp_f32 v96, v96\n" \
    "v_exp_f32 v97, v97\n" \
    "v_cvt_pk_bf16_f32 v134, v124, v125\n" \
    "v_cvt_pk_bf16_f32 v135, v126, v127\n" \
    "v_exp_f32 v98, v98\n" \
    "v_exp_f32 v99, v99\n" \
    "v_mfma_f32_32x32x16_bf16 v[112:127], v[136:139], v[88:91], 0\n" \
    "v_exp_f32 v100, v100\n" \
    "v_exp_f32 v101, v101\n" \
    "v_cvt_pk_bf16_f32 v128, v96, v97\n" \
    "v_cvt_pk_bf16_f32 v129, v98, v99\n" \
    "v_exp_f32 v102, v102\n" \
    "v_exp_f32 v103, v103\n" \
    "v_mfma_f32_32x32x16_bf16 v[112:127], v[140:143], v[92:95], v[112:127]\n" \
    "v_exp_f32 v104, v104\n" \
    "v_exp_f32 v105, v105\n" \
    "v_cvt_pk_bf16_f32 v130, v100, v101\n" \
    "v_cvt_pk_bf16_f32 v131, v102, v103\n" \
    "v_exp_f32 v106, v106\n" \
    "v_exp_f32 v107, v107\n" \
    "v_mfma_f32_32x32x16_bf16 v[16:31], v[172:175], v[132:135], v[16:31]\n" \
    "v_exp_f32 v108, v108\n" \
    "v_exp_f32 v109, v109\n" \
    "v_cvt_pk_bf16_f32 v132, v104, v105\n" \
    "v_cvt_pk_bf16_f32 v133, v106, v107\n" \
    "v_exp_f32 v110, v110\n" \
    "v_exp_f32 v111, v111\n" \
    "v_mfma_f32_32x32x16_bf16 v[32:47], v[168:171], v[128:131], v[32:47]\n" \
    "v_exp_f32 v112, v112\n" \
    "v_exp_f32 v113, v113\n" \
    "v_cvt_pk_bf16_f32 v134, v108, v109\n" \
    "v_cvt_pk_bf16_f32 v135, v110, v111\n" \
    "v_exp_f32 v114, v114\n" \
    "v_exp_f32 v115, v115\n" \
    "v_mfma_f32_32x32x16_bf16 v[96:111], v[144:147], v[64:67], 0\n" \
    "v_exp_f32 v116, v116\n" \
    "v_exp_f32 v117, v117\n" \
    "v_cvt_pk_bf16_f32 v128, v112, v113\n" \
    "v_cvt_pk_bf16_f32 v129, v114, v115\n" \
    "v_exp_f32 v118, v118\n" \
    "v_exp_f32 v119, v119\n" \
    "v_mfma_f32_32x32x16_bf16 v[96:111], v[148:151], v[68:71], v[96:111]\n" \
    "v_exp_f32 v120, v120\n" \
    "v_exp_f32 v121, v121\n" \
    "v_cvt_pk_bf16_f32 v130, v116, v117\n" \
    "v_cvt_pk_bf16_f32 v131, v118, v119\n" \
    "v_exp_f32 v122, v122\n" \
    "v_exp_f32 v123, v123\n" \
    "v_mfma_f32_32x32x16_bf16 v[32:47], v[172:175], v[132:135], v[32:47]\n" \
    "v_exp_f32 v124, v124\n" \
    "v_exp_f32 v125, v125\n" \
    "v_cvt_pk_bf16_f32 v132, v120, v121\n" \
    "v_cvt_pk_bf16_f32 v133, v122, v123\n" \
    "v_exp_f32 v126, v126\n" \
    "v_exp_f32 v127, v127\n" \
    "v_mfma_f32_32x32x16_bf16 v[48:63], v[168:171], v[128:131], v[48:63]\n" \
    "s_waitcnt vmcnt(6)\n" \
    "v_exp_f32 v96, v96\n" \
    "v_exp_f32 v97, v97\n" \
    "v_cvt_pk_bf16_f32 v134, v124, v125\n" \
    "v_cvt_pk_bf16_f32 v135, v126, v127\n" \
    "v_exp_f32 v98, v98\n" \
    "v_exp_f32 v99, v99\n" \
    "v_mfma_f32_32x32x16_bf16 v[112:127], v[152:155], v[64:67], 0\n" \
    "v_exp_f32 v100, v100\n" \
    "v_exp_f32 v101, v101\n" \
    "v_cvt_pk_bf16_f32 v128, v96, v97\n" \
    "v_cvt_pk_bf16_f32 v129, v98, v99\n" \
    "v_exp_f32 v102, v102\n" \
    "v_exp_f32 v103, v103\n" \
    "v_mfma_f32_32x32x16_bf16 v[112:127], v[156:159], v[68:71], v[112:127]\n" \
    "v_exp_f32 v104, v104\n" \
    "v_exp_f32 v105, v105\n" \
    "v_cvt_pk_bf16_f32 v130, v100, v101\n" \
    "v_cvt_pk_bf16_f32 v131, v102, v103\n" \
    "v_exp_f32 v106, v106\n" \
    "v_exp_f32 v107, v107\n" \
    "v_mfma_f32_32x32x16_bf16 v[48:63], v[172:175], v[132:135], v[48:63]\n" \
    "v_exp_f32 v108, v108\n" \
    "v_exp_f32 v109, v109\n" \
    "v_cvt_pk_bf16_f32 v132, v104, v105\n" \
    "v_cvt_pk_bf16_f32 v133, v106, v107\n" \
    "v_exp_f32 v110, v110\n" \
    "v_exp_f32 v111, v111\n" \
    "v_mfma_f32_32x32x16_bf16 v[0:15], v[176:179], v[128:131], v[0:15]\n" \
    "s_waitcnt vmcnt(4)\n" \
    "v_exp_f32 v112, v112\n" \
    "v_exp_f32 v113, v113\n" \
    "v_cvt_pk_bf16_f32 v134, v108, v109\n" \
    "v_cvt_pk_bf16_f32 v135, v110, v111\n" \
    "v_exp_f32 v114, v114\n" \
    "v_exp_f32 v115, v115\n" \
    "v_mfma_f32_32x32x16_bf16 v[96:111], v[160:163], v[64:67], 0\n" \
    "v_exp_f32 v116, v116\n" \
    "v_exp_f32 v117, v117\n" \
    "v_cvt_pk_bf16_f32 v128, v112, v113\n" \
    "v_cvt_pk_bf16_f32 v129, v114, v115\n" \
    "v_exp_f32 v118, v118\n" \
    "v_exp_f32 v119, v119\n" \
    "v_mfma_f32_32x32x16_bf16 v[96:111], v[164:167], v[68:71], v[96:111]\n" \
    "s_waitcnt vmcnt(2)\n" \
    "v_exp_f32 v120, v120\n" \
    "v_exp_f32 v121, v121\n" \
    "v_cvt_pk_bf16_f32 v130, v116, v117\n" \
    "v_cvt_pk_bf16_f32 v131, v118, v119\n" \
    "v_exp_f32 v122, v122\n" \
    "v_exp_f32 v123, v123\n" \
    "v_mfma_f32_32x32x16_bf16 v[0:15], v[180:183], v[132:135], v[0:15]\n" \
    "v_exp_f32 v124, v124\n" \
    "v_exp_f32 v125, v125\n" \
    "v_cvt_pk_bf16_f32 v132, v120, v121\n" \
    "v_cvt_pk_bf16_f32 v133, v122, v123\n" \
    "v_exp_f32 v126, v126\n" \
    "v_exp_f32 v127, v127\n" \
    "v_mfma_f32_32x32x16_bf16 v[0:15], v[184:187], v[128:131], v[0:15]\n" \
    "v_exp_f32 v96, v96\n" \
    "v_exp_f32 v97, v97\n" \
    "v_cvt_pk_bf16_f32 v134, v124, v125\n" \
    "v_cvt_pk_bf16_f32 v135, v126, v127\n" \
    "v_exp_f32 v98, v98\n" \
    "v_exp_f32 v99, v99\n" \
    "v_mfma_f32_32x32x16_bf16 v[112:127], v[144:147], v[72:75], 0\n" \
    "buffer_load_dwordx2 v[200:201], v240, s[40:43], 0 offen sc1\n" \
    "v_exp_f32 v100, v100\n" \
    "v_exp_f32 v101, v101\n" \
    "v_cvt_pk_bf16_f32 v128, v96, v97\n" \
    "v_cvt_pk_bf16_f32 v129, v98, v99\n" \
    "v_exp_f32 v102, v102\n" \
    "v_exp_f32 v103, v103\n" \
    "v_mfma_f32_32x32x16_bf16 v[112:127], v[148:151], v[76:79], v[112:127]\n" \
    "s_waitcnt vmcnt(1)\n" \
    "v_exp_f32 v104, v104\n" \
    "v_exp_f32 v105, v105\n" \
    "v_cvt_pk_bf16_f32 v130, v100, v101\n" \
    "v_cvt_pk_bf16_f32 v131, v102, v103\n" \
    "v_exp_f32 v106, v106\n" \
    "v_exp_f32 v107, v107\n" \
    "v_mfma_f32_32x32x16_bf16 v[0:15], v[188:191], v[132:135], v[0:15]\n" \
    "v_exp_f32 v108, v108\n" \
    "v_exp_f32 v109, v109\n" \
    "v_cvt_pk_bf16_f32 v132, v104, v105\n" \
    "v_cvt_pk_bf16_f32 v133, v106, v107\n" \
    "v_exp_f32 v110, v110\n" \
    "v_exp_f32 v111, v111\n" \
    "v_mfma_f32_32x32x16_bf16 v[0:15], v[192:195], v[128:131], v[0:15]\n" \
    "v_exp_f32 v112, v112\n" \
    "v_exp_f32 v113, v113\n" \
    "v_cvt_pk_bf16_f32 v134, v108, v109\n" \
    "v_cvt_pk_bf16_f32 v135, v110, v111\n" \
    "v_exp_f32 v114, v114\n" \
    "v_exp_f32 v115, v115\n" \
    "v_mfma_f32_32x32x16_bf16 v[96:111], v[152:155], v[72:75], 0\n" \
    "v_exp_f32 v116, v116\n" \
    "v_exp_f32 v117, v117\n" \
    "v_cvt_pk_bf16_f32 v128, v112, v113\n" \
    "v_cvt_pk_bf16_f32 v129, v114, v115\n" \
    "v_exp_f32 v118, v118\n" \
    "v_exp_f32 v119, v119\n" \
    "v_mfma_f32_32x32x16_bf16 v[96:111], v[156:159], v[76:79], v[96:111]\n" \
    "v_exp_f32 v120, v120\n" \
    "v_exp_f32 v121, v121\n" \
    "v_cvt_pk_bf16_f32 v130, v116, v117\n" \
    "v_cvt_pk_bf16_f32 v131, v118, v119\n" \
    "v_exp_f32 v122, v122\n" \
    "v_exp_f32 v123, v123\n" \
    "v_mfma_f32_32x32x16_bf16 v[0:15], v[196:199], v[132:135], v[0:15]\n" \
    "v_exp_f32 v124, v124\n" \
    "v_exp_f32 v125, v125\n" \
    "v_cvt_pk_bf16_f32 v132, v120, v121\n" \
    "v_cvt_pk_bf16_f32 v133, v122, v123\n" \
    "v_exp_f32 v126, v126\n" \
    "v_exp_f32 v127, v127\n" \
    "v_mfma_f32_32x32x16_bf16 v[16:31], v[176:179], v[128:131], v[16:31]\n" \
    "v_exp_f32 v96, v96\n" \
    "v_exp_f32 v97, v97\n" \
    "v_cvt_pk_bf16_f32 v134, v124, v125\n" \
    "v_cvt_pk_bf16_f32 v135, v126, v127\n" \
    "v_exp_f32 v98, v98\n" \
    "v_exp_f32 v99, v99\n" \
    "v_mfma_f32_32x32x16_bf16 v[112:127], v[160:163], v[72:75], 0\n" \
    "ds_write_b128 v229, v[0:3] offset:0\n" \
    "v_exp_f32 v100, v100\n" \
    "v_exp_f32 v101, v101\n" \
    "v_cvt_pk_bf16_f32 v128, v96, v97\n" \
    "v_cvt_pk_bf16_f32 v129, v98, v99\n" \
    "v_exp_f32 v102, v102\n" \
    "v_exp_f32 v103, v103\n" \
    "v_mfma_f32_32x32x16_bf16 v[112:127], v[164:167], v[76:79], v[112:127]\n" \
    "ds_write_b128 v229, v[4:7] offset:1024\n" \
    "v_exp_f32 v104, v104\n" \
    "v_exp_f32 v105, v105\n" \
    "v_cvt_pk_bf16_f32 v130, v100, v101\n" \
    "v_cvt_pk_bf16_f32 v131, v102, v103\n" \
    "v_exp_f32 v106, v106\n" \
    "v_exp_f32 v107, v107\n" \
    "v_mfma_f32_32x32x16_bf16 v[16:31], v[180:183], v[132:135], v[16:31]\n" \
    "ds_write_b128 v229, v[8:11] offset:2048\n" \
    "v_exp_f32 v108, v108\n" \
    "v_exp_f32 v109, v109\n" \
    "v_cvt_pk_bf16_f32 v132, v104, v105\n" \
    "v_cvt_pk_bf16_f32 v133, v106, v107\n" \
    "v_exp_f32 v110, v110\n" \
    "v_exp_f32 v111, v111\n" \
    "v_mfma_f32_32x32x16_bf16 v[16:31], v[184:187], v[128:131], v[16:31]\n" \
    "ds_write_b128 v229, v[12:15] offset:3072\n" \
    "v_exp_f32 v112, v112\n" \
    "v_exp_f32 v113, v113\n" \
    "v_cvt_pk_bf16_f32 v134, v108, v109\n" \
    "v_cvt_pk_bf16_f32 v135, v110, v111\n" \
    "v_exp_f32 v114, v114\n" \
    "v_exp_f32 v115, v115\n" \
    "v_mfma_f32_32x32x16_bf16 v[96:111], v[144:147], v[80:83], 0\n" \
    "v_exp_f32 v116, v116\n" \
    "v_exp_f32 v117, v117\n" \
    "v_cvt_pk_bf16_f32 v128, v112, v113\n" \
    "v_cvt_pk_bf16_f32 v129, v114, v115\n" \
    "v_exp_f32 v118, v118\n" \
    "v_exp_f32 v119, v119\n" \
    "v_mfma_f32_32x32x16_bf16 v[96:111], v[148:151], v[84:87], v[96:111]\n" \
    "v_exp_f32 v120, v120\n" \
    "v_exp_f32 v121, v121\n" \
    "v_cvt_pk_bf16_f32 v130, v116, v117\n" \
    "v_cvt_pk_bf16_f32 v131, v118, v119\n" \
    "v_exp_f32 v122, v122\n" \
    "v_exp_f32 v123, v123\n" \
    "v_mfma_f32_32x32x16_bf16 v[16:31], v[188:191], v[132:135], v[16:31]\n" \
    "v_exp_f32 v124, v124\n" \
    "v_exp_f32 v125, v125\n" \
    "v_cvt_pk_bf16_f32 v132, v120, v121\n" \
    "v_cvt_pk_bf16_f32 v133, v122, v123\n" \
    "v_exp_f32 v126, v126\n" \
    "v_exp_f32 v127, v127\n" \
    "v_mfma_f32_32x32x16_bf16 v[16:31], v[192:195], v[128:131], v[16:31]\n" \
    "v_exp_f32 v96, v96\n" \
    "v_exp_f32 v97, v97\n" \
    "v_cvt_pk_bf16_f32 v134, v124, v125\n" \
    "v_cvt_pk_bf16_f32 v135, v126, v127\n" \
    "v_exp_f32 v98, v98\n" \
    "v_exp_f32 v99, v99\n" \
    "v_mfma_f32_32x32x16_bf16 v[112:127], v[152:155], v[80:83], 0\n" \
    "v_exp_f32 v100, v100\n" \
    "v_exp_f32 v101, v101\n" \
    "v_cvt_pk_bf16_f32 v128, v96, v97\n" \
    "v_cvt_pk_bf16_f32 v129, v98, v99\n" \
    "v_exp_f32 v102, v102\n" \
    "v_exp_f32 v103, v103\n" \
    "v_mfma_f32_32x32x16_bf16 v[112:127], v[156:159], v[84:87], v[112:127]\n" \
    "v_exp_f32 v104, v104\n" \
    "v_exp_f32 v105, v105\n" \
    "v_cvt_pk_bf16_f32 v130, v100, v101\n" \
    "v_cvt_pk_bf16_f32 v131, v102, v103\n" \
    "v_exp_f32 v106, v106\n" \
    "v_exp_f32 v107, v107\n" \
    "v_mfma_f32_32x32x16_bf16 v[16:31], v[196:199], v[132:135], v[16:31]\n" \
    "v_exp_f32 v108, v108\n" \
    "v_exp_f32 v109, v109\n" \
    "v_cvt_pk_bf16_f32 v132, v104, v105\n" \
    "v_cvt_pk_bf16_f32 v133, v106, v107\n" \
    "v_exp_f32 v110, v110\n" \
    "v_exp_f32 v111, v111\n" \
    "v_mfma_f32_32x32x16_bf16 v[32:47], v[176:179], v[128:131], v[32:47]\n" \
    "v_exp_f32 v112, v112\n" \
    "v_exp_f32 v113, v113\n" \
    "v_cvt_pk_bf16_f32 v134, v108, v109\n" \
    "v_cvt_pk_bf16_f32 v135, v110, v111\n" \
    "v_exp_f32 v114, v114\n" \
    "v_exp_f32 v115, v115\n" \
    "v_mfma_f32_32x32x16_bf16 v[96:111], v[160:163], v[80:83], 0\n" \
    "ds_write_b128 v230, v[16:19] offset:0\n" \
    "v_exp_f32 v116, v116\n" \
    "v_exp_f32 v117, v117\n" \
    "v_cvt_pk_bf16_f32 v128, v112, v113\n" \
    "v_cvt_pk_bf16_f32 v129, v114, v115\n" \
    "v_exp_f32 v118, v118\n" \
    "v_exp_f32 v119, v119\n" \
    "v_mfma_f32_32x32x16_bf16 v[96:111], v[164:167], v[84:87], v[96:111]\n" \
    "ds_write_b128 v230, v[20:23] offset:1024\n" \
    "v_exp_f32 v120, v120\n" \
    "v_exp_f32 v121, v121\n" \
    "v_cvt_pk_bf16_f32 v130, v116, v117\n" \
    "v_cvt_pk_bf16_f32 v131, v118, v119\n" \
    "v_exp_f32 v122, v122\n" \
    "v_exp_f32 v123, v123\n" \
    "v_mfma_f32_32x32x16_bf16 v[32:47], v[180:183], v[132:135], v[32:47]\n" \
    "ds_write_b128 v230, v[24:27] offset:2048\n" \
    "v_exp_f32 v124, v124\n" \
    "v_exp_f32 v125, v125\n" \
    "v_cvt_pk_bf16_f32 v132, v120, v121\n" \
    "v_cvt_pk_bf16_f32 v133, v122, v123\n" \
    "v_exp_f32 v126, v126\n" \
    "v_exp_f32 v127, v127\n" \
    "v_mfma_f32_32x32x16_bf16 v[32:47], v[184:187], v[128:131], v[32:47]\n" \
    "ds_write_b128 v230, v[28:31] offset:3072\n" \
    "v_exp_f32 v96, v96\n" \
    "v_exp_f32 v97, v97\n" \
    "v_cvt_pk_bf16_f32 v134, v124, v125\n" \
    "v_cvt_pk_bf16_f32 v135, v126, v127\n" \
    "v_exp_f32 v98, v98\n" \
    "v_exp_f32 v99, v99\n" \
    "v_mfma_f32_32x32x16_bf16 v[112:127], v[144:147], v[88:91], 0\n" \
    "v_exp_f32 v100, v100\n" \
    "v_exp_f32 v101, v101\n" \
    "v_cvt_pk_bf16_f32 v128, v96, v97\n" \
    "v_cvt_pk_bf16_f32 v129, v98, v99\n" \
    "v_exp_f32 v102, v102\n" \
    "v_exp_f32 v103, v103\n" \
    "v_mfma_f32_32x32x16_bf16 v[112:127], v[148:151], v[92:95], v[112:127]\n" \
    "v_exp_f32 v104, v104\n" \
    "v_exp_f32 v105, v105\n" \
    "v_cvt_pk_bf16_f32 v130, v100, v101\n" \
    "v_cvt_pk_bf16_f32 v131, v102, v103\n" \
    "v_exp_f32 v106, v106\n" \
    "v_exp_f32 v107, v107\n" \
    "v_mfma_f32_32x32x16_bf16 v[32:47], v[188:191], v[132:135], v[32:47]\n" \
    "v_exp_f32 v108, v108\n" \
    "v_exp_f32 v109, v109\n" \
    "v_cvt_pk_bf16_f32 v132, v104, v105\n" \
    "v_cvt_pk_bf16_f32 v133, v106, v107\n" \
    "v_exp_f32 v110, v110\n" \
    "v_exp_f32 v111, v111\n" \
    "v_mfma_f32_32x32x16_bf16 v[32:47], v[192:195], v[128:131], v[32:47]\n" \
    "v_exp_f32 v112, v112\n" \
    "v_exp_f32 v113, v113\n" \
    "v_cvt_pk_bf16_f32 v134, v108, v109\n" \
    "v_cvt_pk_bf16_f32 v135, v110, v111\n" \
    "v_exp_f32 v114, v114\n" \
    "v_exp_f32 v115, v115\n" \
    "v_mfma_f32_32x32x16_bf16 v[96:111], v[152:155], v[88:91], 0\n" \
    "v_exp_f32 v116, v116\n" \
    "v_exp_f32 v117, v117\n" \
    "v_cvt_pk_bf16_f32 v128, v112, v113\n" \
    "v_cvt_pk_bf16_f32 v129, v114, v115\n" \
    "v_exp_f32 v118, v118\n" \
    "v_exp_f32 v119, v119\n" \
    "v_mfma_f32_32x32x16_bf16 v[96:111], v[156:159], v[92:95], v[96:111]\n" \
    "v_exp_f32 v120, v120\n" \
    "v_exp_f32 v121, v121\n" \
    "v_cvt_pk_bf16_f32 v130, v116, v117\n" \
    "v_cvt_pk_bf16_f32 v131, v118, v119\n" \
    "v_exp_f32 v122, v122\n" \
    "v_exp_f32 v123, v123\n" \
    "v_mfma_f32_32x32x16_bf16 v[32:47], v[196:199], v[132:135], v[32:47]\n" \
    "v_exp_f32 v124, v124\n" \
    "v_exp_f32 v125, v125\n" \
    "v_cvt_pk_bf16_f32 v132, v120, v121\n" \
    "v_cvt_pk_bf16_f32 v133, v122, v123\n" \
    "v_exp_f32 v126, v126\n" \
    "v_exp_f32 v127, v127\n" \
    "v_mfma_f32_32x32x16_bf16 v[48:63], v[176:179], v[128:131], v[48:63]\n" \
    "v_exp_f32 v96, v96\n" \
    "v_exp_f32 v97, v97\n" \
    "v_cvt_pk_bf16_f32 v134, v124, v125\n" \
    "v_cvt_pk_bf16_f32 v135, v126, v127\n" \
    "v_exp_f32 v98, v98\n" \
    "v_exp_f32 v99, v99\n" \
    "v_mfma_f32_32x32x16_bf16 v[112:127], v[160:163], v[88:91], 0\n" \
    "ds_write_b128 v231, v[32:35] offset:0\n" \
    "v_exp_f32 v100, v100\n" \
    "v_exp_f32 v101, v101\n" \
    "v_cvt_pk_bf16_f32 v128, v96, v97\n" \
    "v_cvt_pk_bf16_f32 v129, v98, v99\n" \
    "v_exp_f32 v102, v102\n" \
    "v_exp_f32 v103, v103\n" \
    "v_mfma_f32_32x32x16_bf16 v[112:127], v[164:167], v[92:95], v[112:127]\n" \
    "s_waitcnt vmcnt(0)\n" \
    "ds_write_b128 v231, v[36:39] offset:1024\n" \
    "v_exp_f32 v104, v104\n" \
    "v_exp_f32 v105, v105\n" \
    "v_cvt_pk_bf16_f32 v130, v100, v101\n" \
    "v_cvt_pk_bf16_f32 v131, v102, v103\n" \
    "v_exp_f32 v106, v106\n" \
    "v_exp_f32 v107, v107\n" \
    "v_mfma_f32_32x32x16_bf16 v[48:63], v[180:183], v[132:135], v[48:63]\n" \
    "ds_write_b128 v231, v[40:43] offset:2048\n" \
    "v_exp_f32 v108, v108\n" \
    "v_exp_f32 v109, v109\n" \
    "v_cvt_pk_bf16_f32 v132, v104, v105\n" \
    "v_cvt_pk_bf16_f32 v133, v106, v107\n" \
    "v_exp_f32 v110, v110\n" \
    "v_exp_f32 v111, v111\n" \
    "v_mfma_f32_32x32x16_bf16 v[48:63], v[184:187], v[128:131], v[48:63]\n" \
    "ds_write_b128 v231, v[44:47] offset:3072\n" \
    "s_mov_b64 exec, 1\n" \
    "ds_add_u32 v232, v203 offset:32\n" \
    "s_mov_b64 exec, -1\n" \
    "v_exp_f32 v112, v112\n" \
    "v_exp_f32 v113, v113\n" \
    "v_cvt_pk_bf16_f32 v134, v108, v109\n" \
    "v_cvt_pk_bf16_f32 v135, v110, v111\n" \
    "v_exp_f32 v114, v114\n" \
    "v_exp_f32 v115, v115\n" \
    "v_exp_f32 v116, v116\n" \
    "v_exp_f32 v117, v117\n" \
    "v_cvt_pk_bf16_f32 v128, v112, v113\n" \
    "v_cvt_pk_bf16_f32 v129, v114, v115\n" \
    "v_exp_f32 v118, v118\n" \
    "v_exp_f32 v119, v119\n" \
    "v_exp_f32 v120, v120\n" \
    "v_exp_f32 v121, v121\n" \
    "v_cvt_pk_bf16_f32 v130, v116, v117\n" \
    "v_cvt_pk_bf16_f32 v131, v118, v119\n" \
    "v_exp_f32 v122, v122\n" \
    "v_exp_f32 v123, v123\n" \
    "v_mfma_f32_32x32x16_bf16 v[48:63], v[188:191], v[132:135], v[48:63]\n" \
    "v_exp_f32 v124, v124\n" \
    "v_exp_f32 v125, v125\n" \
    "v_cvt_pk_bf16_f32 v132, v120, v121\n" \
    "v_cvt_pk_bf16_f32 v133, v122, v123\n" \
    "v_exp_f32 v126, v126\n" \
    "v_exp_f32 v127, v127\n" \
    "v_mfma_f32_32x32x16_bf16 v[48:63], v[192:195], v[128:131], v[48:63]\n" \
    "v_cvt_pk_bf16_f32 v134, v124, v125\n" \
    "v_cvt_pk_bf16_f32 v135, v126, v127\n" \
    "s_mov_b32 s46, 0x35800000\n" \
    "s_mov_b32 s47, 0x67800000\n" \
    "v_mfma_f32_32x32x16_bf16 v[48:63], v[196:199], v[132:135], v[48:63]\n" \
    "s_mov_b32 s45, 0x7b800000\n" \
    "s_mov_b32 %[cnt], 0x200000\n" \
    "LSPS_%=:\n" \
    "ds_read_b32 v202, v232 offset:32\n" \
    "s_sub_u32 %[cnt], %[cnt], 1\n" \
    "s_cbranch_scc1 LGOS_%=\n" \
    "s_waitcnt lgkmcnt(0)\n" \
    "v_cmp_gt_u32 vcc, 4, v202\n" \
    "s_cbranch_vccnz LSPS_%=\n" \
    "LGOS_%=:\n" \
    "s_waitcnt lgkmcnt(0)\n" \
    "ds_read_b128 v[12:15], v233 offset:3072\n" \
    "ds_read_b128 v[0:3], v233 offset:0\n" \
    "ds_read_b128 v[4:7], v233 offset:1024\n" \
    "ds_read_b128 v[8:11], v233 offset:2048\n" \
    "ds_read_b128 v[28:31], v233 offset:7168\n" \
    "ds_read_b128 v[16:19], v233 offset:4096\n" \
    "ds_read_b128 v[20:23], v233 offset:5120\n" \
    "ds_read_b128 v[24:27], v233 offset:6144\n" \
    "ds_read_b128 v[44:47], v233 offset:11264\n" \
    "ds_read_b128 v[32:35], v233 offset:8192\n" \
    "ds_read_b128 v[36:39], v233 offset:9216\n" \
    "ds_read_b128 v[40:43], v233 offset:10240\n" \
    "s_waitcnt lgkmcnt(8)\n" \
    "v_pk_add_f32 v[0:1], v[0:1], v[48:49]\n" \
    "v_pk_add_f32 v[2:3], v[2:3], v[50:51]\n" \
    "v_pk_add_f32 v[4:5], v[4:5], v[52:53]\n" \
    "v_pk_add_f32 v[6:7], v[6:7], v[54:55]\n" \
    "v_pk_add_f32 v[8:9], v[8:9], v[56:57]\n" \
    "v_pk_add_f32 v[10:11], v[10:11], v[58:59]\n" \
    "v_pk_add_f32 v[12:13], v[12:13], v[60:61]\n" \
    "v_pk_add_f32 v[14:15], v[14:15], v[62:63]\n" \
    "s_waitcnt lgkmcnt(4)\n" \
    "v_pk_add_f32 v[0:1], v[0:1], v[16:17]\n" \
    "v_pk_add_f32 v[2:3], v[2:3], v[18:19]\n" \
    "v_pk_add_f32 v[4:5], v[4:5], v[20:21]\n" \
    "v_pk_add_f32 v[6:7], v[6:7], v[22:23]\n" \
    "v_pk_add_f32 v[8:9], v[8:9], v[24:25]\n" \
    "v_pk_add_f32 v[10:11], v[10:11], v[26:27]\n" \
    "v_pk_add_f32 v[12:13], v[12:13], v[28:29]\n" \
    "v_pk_add_f32 v[14:15], v[14:15], v[30:31]\n" \
    "s_waitcnt lgkmcnt(0)\n" \
    "v_pk_add_f32 v[12:13], v[12:13], v[44:45]\n" \
    "v_pk_add_f32 v[14:15], v[14:15], v[46:47]\n" \
    "v_pk_add_f32 v[0:1], v[0:1], v[32:33]\n" \
    "v_pk_add_f32 v[2:3], v[2:3], v[34:35]\n" \
    "v_pk_add_f32 v[4:5], v[4:5], v[36:37]\n" \
    "v_pk_add_f32 v[6:7], v[6:7], v[38:39]\n" \
    "v_pk_add_f32 v[8:9], v[8:9], v[40:41]\n" \
    "v_pk_add_f32 v[10:11], v[10:11], v[42:43]\n" \
    "v_mov_b32 v64, v12\n" \
    "v_mov_b32 v65, v12\n" \
    "v_add_f32 v16, |v0|, |v1|\n" \
    "v_add_f32 v17, |v2|, |v3|\n" \
    "v_add_f32 v18, |v4|, |v5|\n" \
    "v_add_f32 v19, |v6|, |v7|\n" \
    "v_add_f32 v20, |v8|, |v9|\n" \
    "v_add_f32 v21, |v10|, |v11|\n" \
    "v_add_f32 v22, |v12|, |v13|\n" \
    "v_add_f32 v23, |v14|, |v15|\n" \
    "v_permlane32_swap_b32 v64, v65\n" \
    "v_add_f32 v16, v16, v20\n" \
    "v_add_f32 v17, v17, v21\n" \
    "v_add_f32 v18, v18, v22\n" \
    "v_add_f32 v19, v19, v23\n" \
    "v_rcp_f32 v66, v65\n" \
    "v_add_f32 v16, v16, v17\n" \
    "v_add_f32 v18, v18, v19\n" \
    "v_cmp_nle_f32 s[36:37], s46, v65\n" \
    "v_cmp_nge_f32 s[38:39], s47, v65\n" \
    "v_add_f32 v16, v16, v18\n" \
    "v_mov_b32 v67, v66\n" \
    "v_cmp_nge_f32 vcc, s45, v16\n" \
    "v_pk_mul_f32 v[0:1], v[0:1], v[66:67]\n" \
    "v_pk_mul_f32 v[2:3], v[2:3], v[66:67]\n" \
    "v_pk_mul_f32 v[4:5], v[4:5], v[66:67]\n" \
    "v_pk_mul_f32 v[6:7], v[6:7], v[66:67]\n" \
    "v_pk_mul_f32 v[8:9], v[8:9], v[66:67]\n" \
    "v_pk_mul_f32 v[10:11], v[10:11], v[66:67]\n" \
    "v_pk_mul_f32 v[12:13], v[12:13], v[66:67]\n" \
    "v_pk_mul_f32 v[14:15], v[14:15], v[66:67]\n" \
    "s_or_b64 s[36:37], s[36:37], s[38:39]\n" \
    "s_or_b64 s[36:37], s[36:37], vcc\n" \
    "s_cselect_b32 %[bad], 17, 1\n" \
    "v_cvt_pk_bf16_f32 v16, v0, v1\n" \
    "v_cvt_pk_bf16_f32 v17, v2, v3\n" \
    "v_cvt_pk_bf16_f32 v18, v4, v5\n" \
    "v_cvt_pk_bf16_f32 v19, v6, v7\n" \
    "v_cvt_pk_bf16_f32 v20, v8, v9\n" \
    "v_cvt_pk_bf16_f32 v21, v10, v11\n" \
    "v_cvt_pk_bf16_f32 v22, v12, v13\n" \
    "v_cvt_pk_bf16_f32 v23, v14, v15\n" \
    "s_mov_b32 exec_lo, 0\n" \
    "v_mov_b64 v[22:23], 0\n" \
    "s_mov_b32 exec_lo, -1\n" \
    "v_min_u32 v16, -2, v16\n" \
    "v_min_u32 v17, -2, v17\n" \
    "v_min_u32 v18, -2, v18\n" \
    "v_min_u32 v19, -2, v19\n" \
    "v_min_u32 v20, -2, v20\n" \
    "v_min_u32 v21, -2, v21\n" \
    "v_min_u32 v22, -2, v22\n" \
    "v_min_u32 v23, -2, v23\n" \
    "s_cbranch_scc1 LNO_%=\n" \
    "buffer_store_dwordx4 v[16:19], v234, s[40:43], 0 offen sc1\n" \
    "buffer_store_dwordx4 v[20:23], v234, s[40:43], 0 offen offset:1024 sc1\n" \
    "LNO_%=:\n" \
    "s_branch LEND_%=\n" \
    "LMID_%=:\n" \
    "s_mov_b32 s47, 0x20000\n" \
    "LPLM_%=:\n" \
    "buffer_load_dwordx2 v[200:201], v238, s[40:43], 0 offen sc1\n" \
    "s_sub_u32 s47, s47, 1\n" \
    "s_cbranch_scc1 LPDM_%=\n" \
    "s_waitcnt vmcnt(0)\n" \
    "v_cmp_ne_u64 vcc, v[200:201], %[tag]\n" \
    "s_cbranch_vccz LPDM_%=\n" \
    "s_sleep 6\n" \
    "s_branch LPLM_%=\n" \
    "LPDM_%=:\n" \
    "s_waitcnt vmcnt(0)\n" \
    "s_branch LUP_%=\n" \
    "LX_%=:\n" \
    "s_mov_b64 exec, -1\n" \
    "v_mov_b32 v202, s80\n" \
    "s_mov_b32 %[cnt], 0x20000\n" \
    "s_branch LGLH_%=\n" \
    "LGPH_%=:\n" \
    "v_cmp_ne_u64 vcc, v[208:209], %[tag]\n" \
    "s_cbranch_vccz LGDH_%=\n" \
    "s_sub_u32 %[cnt], %[cnt], 1\n" \
    "s_cbranch_scc1 LGDH_%=\n" \
    "s_sleep 4\n" \
    "LGLH_%=:\n" \
    "global_load_dwordx2 v[208:209], v202, s[62:63] sc1\n" \
    "s_waitcnt vmcnt(0)\n" \
    "s_branch LGPH_%=\n" \
    "LGDH_%=:\n" \
    "s_branch LRT_%=\n" \
    "LBAD_%=:\n" \
    "s_sub_u32 s45, s45, 1\n" \
    "s_cbranch_scc1 LFB_%=\n" \
    "s_sleep 3\n" \
    "LRT_%=:\n" \
    "buffer_load_dwordx4 v[136:139], v236, s[40:43], 0 offen offset:0 sc1\n" \
    "buffer_load_dwordx4 v[140:143], v206, s[40:43], 0 offen offset:0 sc1\n" \
    "s_mov_b64 exec, s[48:49]\n" \
    "buffer_load_dwordx4 v[168:171], v237, s[40:43], 0 offen offset:0 sc1\n" \
    "buffer_load_dwordx4 v[172:175], v207, s[40:43], 0 offen offset:0 sc1\n" \
    "s_mov_b64 exec, -1\n" \
    "buffer_load_dwordx4 v[144:147], v236, s[40:43], 0 offen offset:512 sc1\n" \
    "buffer_load_dwordx4 v[148:151], v206, s[40:43], 0 offen offset:512 sc1\n" \
    "s_waitcnt vmcnt(0)\n" \
    "s_branch LVA_%=\n" \
    "LFB_%=:\n" \
    "s_mov_b32 s47, 0x20000\n" \
    "LPLF_%=:\n" \
    "buffer_load_dwordx2 v[200:201], v238, s[40:43], 0 offen sc1\n" \
    "s_sub_u32 s47, s47, 1\n" \
    "s_cbranch_scc1 LPDF_%=\n" \
    "s_waitcnt vmcnt(0)\n" \
    "v_cmp_ne_u64 vcc, v[200:201], %[tag]\n" \
    "s_cbranch_vccz LPDF_%=\n" \
    "s_sleep 6\n" \
    "s_branch LPLF_%=\n" \
    "LPDF_%=:\n" \
    "s_waitcnt vmcnt(0)\n" \
    "buffer_load_dwordx4 v[136:139], v236, s[40:43], 0 offen offset:0 sc1\n" \
    "buffer_load_dwordx4 v[140:143], v206, s[40:43], 0 offen offset:0 sc1\n" \
    "s_mov_b64 exec, s[48:49]\n" \
    "buffer_load_dwordx4 v[168:171], v237, s[40:43], 0 offen offset:0 sc1\n" \
    "buffer_load_dwordx4 v[172:175], v207, s[40:43], 0 offen offset:0 sc1\n" \
    "s_mov_b64 exec, -1\n" \
    "buffer_load_dwordx4 v[144:147], v236, s[40:43], 0 offen offset:512 sc1\n" \
    "buffer_load_dwordx4 v[148:151], v206, s[40:43], 0 offen offset:512 sc1\n" \
    "s_mov_b32 s45, 1\n" \
    "s_waitcnt vmcnt(0)\n" \
    "s_branch LA1_%=\n" \
    "LEND_%=:\n"
#define A_CLOB "v0","v1","v2","v3","v4","v5","v6","v7","v8","v9","v10","v11","v12","v13","v14","v15","v16","v17","v18","v19","v20","v21","v22","v23","v24","v25","v26","v27","v28","v29","v30","v31","v32","v33","v34","v35","v36","v37","v38","v39","v40","v41","v42","v43","v44","v45","v46","v47","v48","v49","v50","v51","v52","v53","v54","v55","v56","v57","v58","v59","v60","v61","v62","v63","v64","v65","v66","v67","v68","v69","v70","v71","v72","v73","v74","v75","v76","v77","v78","v79","v80","v81","v82","v83","v84","v85","v86","v87","v88","v89","v90","v91","v92","v93","v94","v95","v96","v97","v98","v99","v100","v101","v102","v103","v104","v105","v106","v107","v108","v109","v110","v111","v112","v113","v114","v115","v116","v117","v118","v119","v120","v121","v122","v123","v124","v125","v126","v127","v128","v129","v130","v131","v132","v133","v134","v135","v136","v137","v138","v139","v140","v141","v142","v143","v144","v145","v146","v147","v148","v149","v150","v151","v152","v153","v154","v155","v156","v157","v158","v159","v160","v161","v162","v163","v164","v165","v166","v167","v168","v169","v170","v171","v172","v173","v174","v175","v176","v177","v178","v179","v180","v181","v182","v183","v184","v185","v186","v187","v188","v189","v190","v191","v192","v193","v194","v195","v196","v197","v198","v199","v202","v203","v204","v205","v206","v207","v208","v209","v210","v211","v212","v213","v214","v215","v216","v217","v218","v219","v220","v221","v222","v223","v224","v225","v226","v227","v228","v229","v230","v231","v232","v233","v234","v235","v236","v237","v238","v239","v240","s34","s35","s36","s37","s38","s39","s40","s41","s42","s43","s44","s45","s46","s47","s48","s49","s50","s51","s52","s53","s54","s55","s56","s57","s58","s59","s60","s61","s80","s81","s82","s83","s84","s85","s86","s87","s88","s89","s90","s91"
#define TAKE_ASM \
    "buffer_load_dwordx4 v[0:3], %[oa], %[rs], 0 offen offset:0 sc1\n" \
    "buffer_load_dwordx4 v[4:7], %[oa], %[rs], 0 offen offset:1024 sc1\n" \
    "buffer_load_dwordx4 v[8:11], %[oa], %[rs], 0 offen offset:2048 sc1\n" \
    "buffer_load_dwordx4 v[12:15], %[oa], %[rs], 0 offen offset:3072 sc1\n" \
    "buffer_load_dwordx4 v[16:19], %[oa], %[rs], %[k4] offen offset:0 sc1\n" \
    "buffer_load_dwordx4 v[20:23], %[oa], %[rs], %[k4] offen offset:1024 sc1\n" \
    "buffer_load_dwordx4 v[24:27], %[oa], %[rs], %[k4] offen offset:2048 sc1\n" \
    "buffer_load_dwordx4 v[28:31], %[oa], %[rs], %[k4] offen offset:3072 sc1\n" \
    "s_waitcnt vmcnt(7)\n" \
    "ds_write_b128 %[la], v[0:3] offset:0\n" \
    "v_max3_u32 v32, v0, v1, v2\n" \
    "v_mov_b32 v33, v3\n" \
    "s_waitcnt vmcnt(6)\n" \
    "ds_write_b128 %[la], v[4:7] offset:1024\n" \
    "v_max3_u32 v32, v32, v4, v5\n" \
    "v_max3_u32 v33, v33, v6, v7\n" \
    "s_waitcnt vmcnt(5)\n" \
    "ds_write_b128 %[la], v[8:11] offset:2048\n" \
    "v_max3_u32 v32, v32, v8, v9\n" \
    "v_max3_u32 v33, v33, v10, v11\n" \
    "s_waitcnt vmcnt(4)\n" \
    "ds_write_b128 %[la], v[12:15] offset:3072\n" \
    "v_max3_u32 v32, v32, v12, v13\n" \
    "v_max3_u32 v33, v33, v14, v15\n" \
    "s_waitcnt vmcnt(3)\n" \
    "ds_write_b128 %[la], v[16:19] offset:4096\n" \
    "v_max3_u32 v32, v32, v16, v17\n" \
    "v_max3_u32 v33, v33, v18, v19\n" \
    "s_waitcnt vmcnt(2)\n" \
    "ds_write_b128 %[la], v[20:23] offset:5120\n" \
    "v_max3_u32 v32, v32, v20, v21\n" \
    "v_max3_u32 v33, v33, v22, v23\n" \
    "s_waitcnt vmcnt(1)\n" \
    "ds_write_b128 %[la], v[24:27] offset:6144\n" \
    "v_max3_u32 v32, v32, v24, v25\n" \
    "v_max3_u32 v33, v33, v26, v27\n" \
    "s_waitcnt vmcnt(0)\n" \
    "ds_write_b128 %[la], v[28:31] offset:7168\n" \
    "v_max3_u32 v32, v32, v28, v29\n" \
    "v_max3_u32 v33, v33, v30, v31\n" \
    "v_max_u32 v32, v32, v33\n" \
    "s_mov_b32 %[ok], 0\n" \
    "v_cmp_eq_u32 vcc, -1, v32\n" \
    "s_cbranch_vccnz LD_%=\n" \
    "s_mov_b32 %[ok], 1\n" \
    "LD_%=:\n" \
    "s_waitcnt lgkmcnt(0)\n"
#define TAKE_CLOB "v0","v1","v2","v3","v4","v5","v6","v7","v8","v9","v10","v11","v12","v13","v14","v15","v16","v17","v18","v19","v20","v21","v22","v23","v24","v25","v26","v27","v28","v29","v30","v31","v32","v33"
#define ENTRY_ASM \
    "s_mov_b32 %[m0s], m0\n" \
    "s_mov_b32 m0, %[dst]\n" \
    "s_cmp_eq_u32 %[role], 0\n" \
    "s_cbranch_scc0 LB_%=\n" \
    "s_add_u32 s36, s76, %[off]\n" \
    "s_addc_u32 s37, s77, 0\n" \
    "global_load_lds_dwordx4 %[xo0], s[36:37] offset:0\n" \
    "global_load_lds_dwordx4 %[xo1], s[36:37] offset:1024\n" \
    "global_load_lds_dwordx4 %[xo2], s[36:37] offset:2048\n" \
    "global_load_lds_dwordx4 %[xo3], s[36:37] offset:3072\n" \
    "s_barrier\n" \
    "s_mul_i32 s38, %[off], 1344\n" \
    "s_mov_b32 m0, %[wdst]\n" \
    "s_add_u32 s36, s78, s38\n" \
    "s_addc_u32 s37, s79, 0\n" \
    "global_load_lds_dwordx4 %[vo0], s[36:37]\n" \
    "global_load_lds_dwordx4 %[vo1], s[36:37] offset:1024\n" \
    "s_branch LE_%=\n" \
    "LB_%=:\n" \
    "ds_write_b32 %[zb], %[zero]\n" \
    "s_cmp_eq_u32 %[role], 1\n" \
    "s_cbranch_scc0 LM_%=\n" \
    "global_load_lds_dword %[po], %[g0]\n" \
    "s_add_u32 m0, m0, 256\n" \
    "s_nop 0\n" \
    "global_load_lds_dword %[po], %[g1]\n" \
    "s_add_u32 m0, m0, 256\n" \
    "s_nop 0\n" \
    "global_load_lds_ushort %[co], %[g6]\n" \
    "s_add_u32 m0, m0, 256\n" \
    "s_nop 0\n" \
    "global_load_lds_ushort %[co], %[g7]\n" \
    "s_add_u32 m0, m0, 256\n" \
    "s_nop 0\n" \
    "global_load_lds_dword %[po], %[g2]\n" \
    "s_add_u32 m0, m0, 256\n" \
    "s_nop 0\n" \
    "global_load_lds_dword %[po], %[g3]\n" \
    "s_add_u32 m0, m0, 256\n" \
    "s_nop 0\n" \
    "global_load_lds_dword %[po], %[g4]\n" \
    "s_add_u32 m0, m0, 256\n" \
    "s_nop 0\n" \
    "global_load_lds_dword %[po], %[g5]\n" \
    "s_add_u32 m0, m0, 256\n" \
    "s_nop 0\n" \
    "s_mov_b64 exec, 1\n" \
    "global_load_lds_dwordx4 %[eo], s[62:63] sc1\n" \
    "s_mov_b64 exec, -1\n" \
    "s_sub_u32 m0, %[dst], 4096\n" \
    "s_add_u32 s36, s76, 13312\n" \
    "s_addc_u32 s37, s77, 0\n" \
    "global_load_lds_dwordx4 %[ub], s[76:77]\n" \
    "global_load_lds_dwordx4 %[ub], s[36:37] offset:1024\n" \
    "s_waitcnt lgkmcnt(0)\n" \
    "s_barrier\n" \
    "s_branch LE_%=\n" \
    "LM_%=:\n" \
    "global_load_dwordx2 %[ev], %[eo], s[62:63] sc1\n" \
    "s_mov_b32 exec_hi, 0xffffff\n" \
    "global_load_lds_dwordx4 %[l16], %[gw]\n" \
    "s_mov_b32 exec_hi, -1\n" \
    "s_sub_u32 m0, m0, 4096\n" \
    "s_add_u32 s36, %[wol], 0\n" \
    "s_addc_u32 s37, %[woh], 0\n" \
    "global_load_lds_dwordx4 %[wov], s[36:37] offset:0\n" \
    "s_add_u32 s36, %[wol], 6144\n" \
    "s_addc_u32 s37, %[woh], 0\n" \
    "global_load_lds_dwordx4 %[wov], s[36:37] offset:1024\n" \
    "s_add_u32 s36, %[wol], 12288\n" \
    "s_addc_u32 s37, %[woh], 0\n" \
    "global_load_lds_dwordx4 %[wov], s[36:37] offset:2048\n" \
    "s_mov_b32 exec_hi, 0\n" \
    "s_add_u32 s36, %[wol], 18432\n" \
    "s_addc_u32 s37, %[woh], 0\n" \
    "global_load_lds_dwordx4 %[wov], s[36:37] offset:3072\n" \
    "s_mov_b32 exec_hi, -1\n" \
    "s_add_u32 s36, s62, 0\n" \
    "s_addc_u32 s37, s63, 0\n" \
    "global_store_dwordx4 %[mo], %[ones], s[36:37] sc1\n" \
    "global_store_dwordx4 %[mo], %[ones], s[36:37] offset:1024 sc1\n" \
    "s_add_u32 s36, s62, 33280\n" \
    "s_addc_u32 s37, s63, 0\n" \
    "global_store_dwordx4 %[mo], %[ones], s[36:37] sc1\n" \
    "global_store_dwordx4 %[mo], %[ones], s[36:37] offset:1024 sc1\n" \
    "s_add_u32 s36, s62, 66560\n" \
    "s_addc_u32 s37, s63, 0\n" \
    "global_store_dwordx4 %[mo], %[ones], s[36:37] sc1\n" \
    "global_store_dwordx4 %[mo], %[ones], s[36:37] offset:1024 sc1\n" \
    "s_add_u32 s36, s62, 99840\n" \
    "s_addc_u32 s37, s63, 0\n" \
    "global_store_dwordx4 %[mo], %[ones], s[36:37] sc1\n" \
    "global_store_dwordx4 %[mo], %[ones], s[36:37] offset:1024 sc1\n" \
    "s_waitcnt lgkmcnt(0)\n" \
    "s_barrier\n" \
    "s_mov_b64 exec, 1\n" \
    "s_waitcnt vmcnt(0)\n" \
    "v_lshl_add_u64 %[ev], %[ev], 0, %[tc]\n" \
    "global_store_dwordx2 %[fm], %[ev], s[62:63] sc1\n" \
    "s_mov_b64 exec, -1\n" \
    "LE_%=:\n" \
    "s_mov_b32 m0, %[m0s]\n"
#define ST_DECL
#define ST(i)
#define ST2(i)
#define STD(n)
#define STS(n)
#define STF(i, n)
#define ST_FLUSH()

DI f32x16 mfma32(u32x4 a, u32x4 b, f32x16 c) { return __builtin_amdgcn_mfma_f32_32x32x16_bf16(__builtin_bit_cast(bf16x8, a), __builtin_bit_cast(bf16x8, b), c, 0, 0, 0); }
DI f32x4 mfma16(u32x4 a, u32x4 b, f32x4 c) { return __builtin_amdgcn_mfma_f32_16x16x32_bf16(__builtin_bit_cast(bf16x8, a), __builtin_bit_cast(bf16x8, b), c, 0, 0, 0); }
DI unsigned pk(float lo, float hi) { f32x2 v = {lo, hi}; return __builtin_bit_cast(unsigned, __builtin_convertvector(v, bf16x2)); }
DI float bflo(unsigned v) { return __uint_as_float(v << 16); }
DI float bfhi(unsigned v) { return __uint_as_float(v & 0xffff0000u); }
template <class T> DI T opq(T p) { asm("" : "+v"(p)); return p; }
DI u32x2 trrd(LAS const char* p) { return __builtin_bit_cast(u32x2, __builtin_amdgcn_ds_read_tr16_b64_v4i16((LAS s16x4*)p)); }
DI u32x4 trrd2(LAS const char* p0, LAS const char* p1) { u32x2 a = trrd(p0), b = trrd(p1); return u32x4{a[0], a[1], b[0], b[1]}; }
DI void dma16(unsigned voff, const void* sbase, unsigned ldsdst) {
    unsigned keep;
    asm volatile("s_mov_b32 %0, m0\n\ts_mov_b32 m0, %3\n\ts_nop 0\n\tglobal_load_lds_dwordx4 %1, %2\n\ts_mov_b32 m0, %0" : "=&s"(keep) : "v"(voff), "s"(sbase), "s"(ldsdst) : "memory");
}
DI f32x2 cmul(f32x2 a, f32x2 b) { float r, i;
    asm("v_mul_f32 %0, %2, %4\n\tv_mul_f32 %1, %2, %5\n\tv_fma_f32 %0, -%3, %5, %0\n\tv_fmac_f32 %1, %3, %4" : "=&v"(r), "=&v"(i) : "v"(a[0]), "v"(a[1]), "v"(b[0]), "v"(b[1])); return f32x2{r, i}; }
#define PRIO(x) __builtin_amdgcn_s_setprio(x)
#define BARRIER() asm volatile("s_waitcnt lgkmcnt(0)\n\ts_barrier" ::: "memory")
#define SBAR() __builtin_amdgcn_sched_barrier(0)
#define DRAIN() asm volatile("s_waitcnt vmcnt(0)" ::: "memory")
#define RLX __ATOMIC_RELAXED, __HIP_MEMORY_SCOPE_AGENT
typedef __amdgpu_buffer_rsrc_t rsrc_t;
DI rsrc_t mk_rsrc(char* ws) { asm volatile("" : "+s"(ws)); return __builtin_amdgcn_make_buffer_rsrc(ws, 0, WS_END, 0x00020000); }
DI void st16(rsrc_t r, unsigned off, u32x4 v) { __builtin_amdgcn_raw_buffer_store_b128(v, r, off, 0, 16); }
DI u32x4 ld16(rsrc_t r, unsigned off) { return __builtin_amdgcn_raw_buffer_load_b128(r, off, 0, 16); }
#define POLL_DELAY 12
DI void mark_y(rsrc_t r, int frt, int fct, int j, int lane) {
    const unsigned o = WS_Y + (frt * 450 + fct * 112) * 64 + j * 2048 + lane * 16;
    st16(r, o, u32x4{~0u, ~0u, ~0u, ~0u}); st16(r, j < 3 ? o + 1024 : WS_END, u32x4{~0u, ~0u, ~0u, ~0u}); }
#define FLOOR_OP
constexpr unsigned CODE_BYTES = 39168;
#define F2_DELAY 40
#define WO_PACE 3
#define CONV_G 16
#define US_EARLY 1
#define US_WORD 52
#define Y_RETRY 16
#define TOUCH 3
#define FU_DELAY 0
#define X_WORD 80
#define O_RETRY 6
#define O_DELAY 8
constexpr unsigned SPIN_CAP = 1u << 17;
template <int SL> DI void wait_flags(char* ws, unsigned off, u64 tag) {
#pragma unroll 1
    for (unsigned n = 0; n < SPIN_CAP; ++n) {
        const u64 v = __hip_atomic_load((GAS u64*)(ws + off), RLX);
        if (__all(v == tag)) break;
        __builtin_amdgcn_s_sleep(SL);
    }
    __builtin_amdgcn_fence(__ATOMIC_ACQUIRE, "wavefront");
}
template <int SL> DI void wait_flags(char* ws, unsigned off, u64 tag, u64 seen) { if (!__all(seen == tag)) wait_flags<SL>(ws, off, tag); }
DI void pair_sync(LAS char* words, int me, int val) {
    asm volatile("s_waitcnt lgkmcnt(0)" ::: "memory");
    __hip_atomic_store((LAS int*)(words + me * 4), val, __ATOMIC_RELAXED, __HIP_MEMORY_SCOPE_WORKGROUP);
    for (unsigned n = 0; n < (SPIN_CAP << 4); ++n)
        if (__builtin_amdgcn_readfirstlane(__hip_atomic_load((LAS int*)(words + (me ^ 1) * 4), __ATOMIC_RELAXED, __HIP_MEMORY_SCOPE_WORKGROUP)) >= val) break;
    asm volatile("" ::: "memory");
}

constexpr int L_X = 57344, L_KG = 106496, L_U = 139264, L_Q = 0, L_P = 16384, L_SYNC = 159744, L_BYTES = 163840;
static_assert(RS == 33280 && WS_K == 1u << 20 && WS_O == 8u << 20 && WS_F1 == (12u << 20) + 40960 && WS_F3 == WS_F1 + 53248 && L_X == 57344 && L_U == 139264 && L_P == 16384 && L_SYNC == 159744, "constants inside A_ASM");
constexpr int L_OS = 65536, L_Y = 98304;
constexpr u64 TAG_C = 0x9E3779B97F4A7C15ull;

DI void cnt_add(LAS char* word, int lane) {
    asm volatile("s_waitcnt lgkmcnt(0)" ::: "memory");
    if (lane == 0) __hip_atomic_fetch_add((LAS int*)word, 1, __ATOMIC_RELAXED, __HIP_MEMORY_SCOPE_WORKGROUP);
}
template <int SL = 1> DI void cnt_wait(LAS char* word, int val) {
#pragma unroll 1
    for (unsigned n = 0; n < (SPIN_CAP << 4); ++n) {
        if (__builtin_amdgcn_readfirstlane(__hip_atomic_load((LAS int*)word, __ATOMIC_RELAXED, __HIP_MEMORY_SCOPE_WORKGROUP)) >= val) break;
        __builtin_amdgcn_s_sleep(SL);
    }
    asm volatile("" ::: "memory");
}
DI void dma_x4(const unsigned (&v)[4], const void* sbase, unsigned ldsdst) {
    unsigned keep;
    asm volatile("s_mov_b32 %0, m0\n\ts_mov_b32 m0, %6\n\ts_nop 0\n\tglobal_load_lds_dwordx4 %1, %5\n\tglobal_load_lds_dwordx4 %2, %5 offset:1024\n\t"
                 "global_load_lds_dwordx4 %3, %5 offset:2048\n\tglobal_load_lds_dwordx4 %4, %5 offset:3072\n\ts_mov_b32 m0, %0"
                 : "=&s"(keep) : "v"(v[0]), "v"(v[1]), "v"(v[2]), "v"(v[3]), "s"(sbase), "s"(ldsdst) : "memory");
}
DI void dma_x2(const unsigned (&v)[2], const void* sbase, unsigned ldsdst) {
    unsigned keep;
    asm volatile("s_mov_b32 %0, m0\n\ts_mov_b32 m0, %4\n\ts_nop 0\n\tglobal_load_lds_dwordx4 %1, %3\n\tglobal_load_lds_dwordx4 %2, %3 offset:1024\n\ts_mov_b32 m0, %0"
                 : "=&s"(keep) : "v"(v[0]), "v"(v[1]), "s"(sbase), "s"(ldsdst) : "memory");
}
DI void dma16o(unsigned voff, const void* sbase, unsigned ldsdst, int i) {
    unsigned keep;
    asm volatile("s_mov_b32 %0, m0\n\ts_mov_b32 m0, %3\n\ts_nop 0\n\tglobal_load_lds_dwordx4 %1, %2 offset:%4\n\ts_mov_b32 m0, %0" : "=&s"(keep) : "v"(voff), "s"(sbase), "s"(ldsdst), "i"(i * 1024) : "memory");
}
template <int N> DI void wait_vm() { asm volatile("s_waitcnt vmcnt(%0)" :: "i"(N) : "memory"); }

DI u64 proj_wave(const u16* x, const u16* wqkv, char* ws, LAS char* lds, int w, int lane, int rt, int hj, int e, int sg, int g0, int hd, const unsigned (&xo)[4], unsigned vo0, int frt, int& bad, u64& f3, unsigned t_start) {
    unsigned vo1 = vo0 + 8 * 2688 - 1024;
    u64 tag; int cnt, m0s;
    asm volatile(A_ASM
        : [tag] "=&v"(tag), [cnt] "=&s"(cnt), [m0s] "=&s"(m0s), [bad] "=&s"(bad), "=&{v[200:201]}"(f3)
        : [xo0] "v"(xo[0]), [xo1] "v"(xo[1]), [xo2] "v"(xo[2]), [xo3] "v"(xo[3]), [eo] "i"(WS_E), [tc] "s"(TAG_C),
          [ring] "s"(L_X + w * 12288), [lent] "s"(L_U + w * 4096), [wdst] "s"(w * 2048), [off] "s"(sg * 128), [mode] "s"(hj < 7 ? e : 2), [wend] "i"(WS_END),
          [ln] "v"(lane), FLOOR_OP [vo0] "v"(vo0), [vo1] "v"(vo1), [w] "s"(w), [rt] "s"(rt), [hj] "s"(hj), [e] "s"(e), [hd] "s"(hd), [g0] "s"(g0), [frt] "s"(frt),
          "{s[76:77]}"(x), "{s[78:79]}"(wqkv), "{s[62:63]}"(ws)
        : "memory", "scc", "vcc", A_CLOB);
    return tag;
}

template <int MODE>
DI void slow_tile(f32x16& o, float& mx, u32x4 q0, u32x4 q1, rsrc_t r, unsigned ko, unsigned vo, int r32) {
    const unsigned one = r32 == 28 ? 0x3f803f80u : 0u; const u32x4 pad = {one, one, one, one};
#pragma unroll 1
    for (int kt = 0; kt < 16; ++kt) {
        f32x16 s = mfma32(ld16(r, ko + kt * 512), q0, f32x16{});
        s = mfma32(ld16(r, ko + kt * 512 + 2 * RS), q1, s);
        if (MODE == 1) {
#pragma unroll
            for (int i = 0; i < 16; ++i) mx = __builtin_fmaxf(mx, s[i]);
        } else {
#pragma unroll
            for (int i = 0; i < 16; ++i) s[i] = __builtin_amdgcn_exp2f(s[i] - mx);
            o = mfma32(r32 < 28 ? ld16(r, vo + kt * 512) : pad, u32x4{pk(s[0], s[1]), pk(s[2], s[3]), pk(s[4], s[5]), pk(s[6], s[7])}, o);
            o = mfma32(r32 < 28 ? ld16(r, vo + kt * 512 + 2 * RS) : pad, u32x4{pk(s[8], s[9]), pk(s[10], s[11]), pk(s[12], s[13]), pk(s[14], s[15])}, o);
        }
    }
}

__global__ __launch_bounds__(512, 2) void mk(const u16* x, const u16* wqkv, const u16* wo, const u16* Are, const u16* Aim, const u16* Bre, const u16* Bim,
                                             const u16* Cre, const u16* Cim, const u16* logdt, const u16* dskip, const u16* gw, char* ws, u16* out) {
    __shared__ __attribute__((aligned(16))) char lds_[L_BYTES];
    LAS char* lds = (LAS char*)lds_;
    ST_DECL; ST(0);
    const unsigned t_start = 0;
    const unsigned lds0 = (unsigned)(uintptr_t)lds;
    int tid = threadIdx.x, lane = tid & 63; const int w = __builtin_amdgcn_readfirstlane(tid >> 6);
    const int b = blockIdx.x, xcd = b & 7, bi = b >> 3;
    {
        int m0s;
        asm volatile("s_mov_b32 %0, m0\n\ts_mov_b32 m0, %3\n\ts_mov_b64 exec, 3\n\tglobal_load_lds_dword %1, %2\n\ts_mov_b64 exec, -1\n\ts_mov_b32 m0, %0"
                     : "=&s"(m0s) : "v"(min((unsigned)(bi * 8 + w + lane * 256) * 128, CODE_BYTES)), "s"(__builtin_amdgcn_s_getpc()), "s"(lds0 + L_SYNC + 512));
    }
    int r32 = lane & 31, hh = lane >> 5, G = lane >> 4, q4 = (lane & 15) >> 2, p4 = lane & 3;
#define RELANE() do { asm volatile("" : "+v"(tid)); lane = tid & 63; r32 = lane & 31; hh = lane >> 5; G = lane >> 4; q4 = (lane & 15) >> 2; p4 = lane & 3; } while (0)
    const int e = xcd & 1, hj = bi >> 2, art = 4 * (xcd >> 1) + (bi & 3), hd = 8 * e + hj, g0 = (7 * hd) >> 1;
    u64 tag = 0;
    int sg = hj + 2 * (bi & 3); sg = sg >= 7 ? sg - 7 : sg;
    const int j = w & 3, c = j >> 1, hf = j & 1, ch = 2 * (xcd * 28 + bi) + c;
    int role = w < 4 ? 0 : hj < 7 ? 1 : 2;
    unsigned xo[4];
    const unsigned vo0 = (16 * j + (lane >> 3)) * 2688 + (((lane & 7) ^ ((G & 1) << 2)) << 4) + (448 * (1 + e) + 64 * min(hj, 6)) * 2;
    {
        const unsigned x0 = (art * 128 + j * 32 + (lane >> 3)) * 896 + (((lane & 7) ^ G) << 4);
#pragma unroll
        for (int i = 0; i < 4; ++i) xo[i] = (x0 ^ ((i & 1) << 6)) + i * (7168 - 1024);
        const unsigned mb = WS_K + (art * 2 + e) * 4 * RS + j * 8192;
        int m0s; u64 ev;
        asm volatile(ENTRY_ASM
            : [ev] "=&v"(ev), [m0s] "=&s"(m0s)
            : [role] "s"(role), [po] "v"((ch * 256 + hf * 128) * 2 + lane * 4), [co] "v"(ch * 2), [eo] "v"(WS_E), [g0] "s"(Are), [g1] "s"(Aim), [g2] "s"(Bre), [g3] "s"(Bim), [g4] "s"(Cre),
              [g5] "s"(Cim), [g6] "s"(logdt), [g7] "s"(dskip), "{s[62:63]}"(ws), "{s[76:77]}"(x), "{s[78:79]}"(wqkv), [off] "s"(sg * 128),
              [dst] "s"(lds0 + (w < 4 ? L_X + j * 12288 + 4096 : L_KG + j * 8192 + 4096)), [wdst] "s"(lds0 + j * 2048),
              [xo0] "v"(xo[0]), [xo1] "v"(xo[1]), [xo2] "v"(xo[2]), [xo3] "v"(xo[3]), [zb] "v"(lds0 + L_SYNC + lane * 4), [zero] "v"(0),
              [vo0] "v"(vo0), [vo1] "v"(vo0 + 8 * 2688 - 1024),
              [ub] "v"((art * 128 + j * 32 + (lane >> 2)) * 896 + sg * 128 + e * 64 + p4 * 16), [mo] "v"(mb + lane * 16), [l16] "v"(lane * 16), [gw] "s"(gw), [wol] "s"((unsigned)(uintptr_t)wo), [woh] "s"((unsigned)((uintptr_t)wo >> 32)),
              [wov] "v"((art * 28 + (lane >> 3)) * 896 + min((2 * e + c) * 224 + hf * 128 + (lane & 7) * 16, 880)), [ones] "v"(u32x4{~0u, ~0u, ~0u, ~0u}), [fm] "v"(WS_FM + ((art * 2 + e) * 4 + j) * 128), [tc] "s"(TAG_C)
            : "memory", "scc", "s36", "s37", "s38");
    }
    SBAR();
    const int fct = bi & 3, frt = xcd * 8 + (bi >> 2);
    if (role == 0) {
        int bad; u64 f3;
        PRIO(3);
        tag = proj_wave(x, wqkv, ws, lds, w, lane, art, hj, e, sg, g0, hd, xo, vo0, frt, bad, f3, t_start);
        RELANE();
        const rsrc_t wr = mk_rsrc(ws);
        if (lane == 0) __hip_atomic_fetch_add((LAS int*)(lds + L_SYNC + 36), bad, __ATOMIC_RELAXED, __HIP_MEMORY_SCOPE_WORKGROUP);
        int v = 0;
#pragma unroll 1
        for (unsigned n = 0; n < (SPIN_CAP << 4); ++n) {
            v = __builtin_amdgcn_readfirstlane(__hip_atomic_load((LAS int*)(lds + L_SYNC + 36), __ATOMIC_RELAXED, __HIP_MEMORY_SCOPE_WORKGROUP));
            if ((v & 15) == 4) break;
        }
        asm volatile("" ::: "memory");
        if (v > 15) {
            cnt_wait<8>(lds + L_SYNC + 60, 4);
            auto fbo = [](int k) { return k < 3 ? L_P + k * 16384 : L_KG; };
            const unsigned ko = WS_K + (g0 + hh) * RS + j * 8192 + r32 * 16, vo = WS_V + (hd * 4 + hh) * RS + j * 8192 + r32 * 16;
#pragma unroll 1
            for (int ps = 1; ps < 3; ++ps) {
#pragma unroll 1
                for (int t = 0; t < 4; ++t) {
                    const u32x4 q0 = *(LAS const u32x4*)(lds + L_Q + t * 2048 + lane * 16), q1 = *(LAS const u32x4*)(lds + L_Q + t * 2048 + 1024 + lane * 16);
                    f32x16 ot = {}; float mx = -__builtin_inff();
                    if (ps == 1) {
                        slow_tile<1>(ot, mx, q0, q1, wr, ko, vo, r32);
                        *(LAS float*)(lds + 8192 + ((t * 4 + w) * 64 + lane) * 4) = mx;
                    } else {
                        for (int k = 0; k < 8; ++k) mx = __builtin_fmaxf(mx, *(LAS const float*)(lds + 8192 + (t * 256 + k * 32 + r32) * 4));
                        slow_tile<2>(ot, mx, q0, q1, wr, ko, vo, r32);
#pragma unroll
                        for (int g = 0; g < 4; ++g) *(LAS f32x4*)(lds + fbo(w) + (t * 4 + g) * 1024 + lane * 16) = f32x4{ot[4 * g], ot[4 * g + 1], ot[4 * g + 2], ot[4 * g + 3]};
                    }
                }
                cnt_add(lds + L_SYNC + 40, lane); cnt_wait(lds + L_SYNC + 40, 4 * ps);
            }
            if (bad > 1) {
                f32x4 a[4] = {};
#pragma unroll
                for (int k = 0; k < 16; ++k) a[k & 3] += *(LAS const f32x4*)(lds + fbo(k >> 2) + w * 4096 + (k & 3) * 1024 + lane * 16);
                const float rl = __builtin_amdgcn_rcpf(__shfl(a[3][0], r32 + 32));
                u32x4 v0 = {pk(a[0][0] * rl, a[0][1] * rl), pk(a[0][2] * rl, a[0][3] * rl), pk(a[1][0] * rl, a[1][1] * rl), pk(a[1][2] * rl, a[1][3] * rl)};
                u32x4 v1 = {pk(a[2][0] * rl, a[2][1] * rl), pk(a[2][2] * rl, a[2][3] * rl), hh ? 0u : pk(a[3][0] * rl, a[3][1] * rl), hh ? 0u : pk(a[3][2] * rl, a[3][3] * rl)};
#pragma unroll
                for (int i = 0; i < 4; ++i) { v0[i] = min(v0[i], 0xfffffffeu); v1[i] = min(v1[i], 0xfffffffeu); }
                const unsigned o = WS_O + ((art * 4 + w) * 16 + hd) * 2048 + lane * 16;
                st16(wr, o, v0); st16(wr, o + 1024, v1);
            }
        }
        ST(21);
        wait_flags<8>(ws, WS_F3 + frt * 128 + (lane & 15) * 8, tag, f3);
        __builtin_amdgcn_s_sleep(O_DELAY);
#pragma unroll 1
        for (unsigned n = 0; n < SPIN_CAP; ++n) {
            int ok;
            asm volatile(TAKE_ASM : [ok] "=&s"(ok)
                : [oa] "v"(WS_O + frt * 32768 + j * 8192 + lane * 16), [la] "v"(lds0 + L_OS + j * 8192 + lane * 16), [rs] "s"(wr), [k4] "s"(4096)
                : "memory", "scc", "vcc", TAKE_CLOB);
            if (ok) break;
            __builtin_amdgcn_s_sleep(O_RETRY);
        }
        BARRIER();
        ST(22);
        ST_FLUSH();
    }
    asm volatile("" : "+s"(role));
    if (role == 0) return;
    PRIO(2);
    asm volatile("" : "+s"(role));
    RELANE();
    if (role == 1) {
        {
            wait_vm<2 + (TOUCH == 1)>();
            LAS const unsigned* pp_ = (LAS const unsigned*)(lds + L_KG + j * 8192 + 4096) + lane;
            const unsigned pAr = pp_[0], pAi = pp_[64], pLd = pp_[128 - lane] & 0xffffu, pDs = pp_[192 - lane] & 0xffffu, pBr = pp_[256], pBi = pp_[320], pCr = pp_[384], pCi = pp_[448];
            tag = *(LAS const u64*)(pp_ + 512 - lane) + TAG_C;
            STD(s1); STD(s2); SBAR(); STS(s1); SBAR();
            const rsrc_t wr = mk_rsrc(ws);
            const float dt = bflo(pk(expf(bflo(pLd)), 0.f));
            LAS char* img = lds + L_KG + j * 8192;
            const int sw = (lane >> 1) & 3, pp = 4 * (G & 1) + p4;
            LAS const char* r0 = opq(img + (8 * hh + q4) * 64 + (((pp >> 1) ^ (q4 >> 1)) << 4) + (pp & 1) * 8);
            LAS const char* r1 = opq(img + (8 * hh + 4 + q4) * 64 + (((pp >> 1) ^ (2 + (q4 >> 1))) << 4) + (pp & 1) * 8);
            u32x4 val[2][14];
            auto pack8 = [&](u32x4& re, u32x4& im, const f32x2 (&v)[8]) {
                re = u32x4{pk(v[0][0], v[1][0]), pk(v[2][0], v[3][0]), pk(v[4][0], v[5][0]), pk(v[6][0], v[7][0])};
                im = u32x4{pk(v[0][1], v[1][1]), pk(v[2][1], v[3][1]), pk(v[4][1], v[5][1]), pk(v[6][1], v[7][1])}; };
#pragma unroll
            for (int p = 0; p < 2; ++p) {
                auto par = [&](unsigned v) { return p ? bfhi(v) : bflo(v); };
                const float ar = dt * par(pAr), ai = dt * par(pAi);
                const float lr = ar * 1.4426950408889634f;
                const float ph = ai * 0.15915494f, pe = __builtin_fmaf(ai, 0.15915494f, -ph) + ai * 6.4206383e-9f;
                auto zpow = [&](float ee) { const float m = __builtin_amdgcn_exp2f(lr * ee); const float t = ph * ee; const float f = (t - __builtin_rintf(t)) + pe * ee;
                                            return f32x2{m * __builtin_amdgcn_cosf(f), m * __builtin_amdgcn_sinf(f)}; };
                f32x2 z1 = zpow(1.f), Z1 = zpow(256.f); const f32x2 z8 = zpow(8.f);
                z1[1] = -z1[1]; Z1[1] = -Z1[1];
                const f32x2 cb = cmul(f32x2{par(pCr), par(pCi)}, f32x2{dt * par(pBr), dt * par(pBi)});
                f32x2 v[8], u[8], t[8];
                v[0] = f32x2{1.f, 0.f}; v[1] = z1; v[2] = cmul(z1, z1); v[3] = cmul(v[2], z1); v[4] = cmul(v[2], v[2]); v[5] = cmul(v[4], z1); v[6] = cmul(v[4], v[2]); v[7] = cmul(v[4], v[3]);
                pack8(val[p][0], val[p][1], v);
                u[0] = Z1; u[1] = cmul(Z1, v[2]); u[2] = cmul(Z1, v[4]); u[3] = cmul(Z1, v[6]); u[4] = cmul(Z1, Z1); u[5] = cmul(u[4], v[4]); u[6] = cmul(u[4], Z1); u[7] = cmul(u[6], v[4]);
                pack8(val[p][2], val[p][3], u);
                t[0] = cmul(u[4], u[4]); t[1] = cmul(t[0], Z1); t[2] = cmul(t[0], u[4]); t[3] = cmul(t[0], u[6]); t[4] = cmul(t[0], t[0]); t[5] = f32x2{0.f, 0.f}; t[6] = t[5]; t[7] = t[5];
                pack8(val[p][4], val[p][5], t);
                const f32x2 z16 = cmul(z8, z8);
                f32x2 e0 = cb, e1 = cmul(cb, z8);
#pragma unroll
                for (int cc = 0; cc < 4; ++cc) {
                    f32x2 a[8];
#pragma unroll
                    for (int i = 0; i < 4; ++i) { a[2 * i] = e0; a[2 * i + 1] = e1; if (cc < 3 || i < 3) { e0 = cmul(e0, z16); e1 = cmul(e1, z16); } }
                    pack8(val[p][6 + 2 * cc], val[p][7 + 2 * cc], a);
                }
                asm volatile("" : "+v"(val[p][0]), "+v"(val[p][1]), "+v"(val[p][2]), "+v"(val[p][3]), "+v"(val[p][4]), "+v"(val[p][5]), "+v"(val[p][6]),
                                  "+v"(val[p][7]), "+v"(val[p][8]), "+v"(val[p][9]), "+v"(val[p][10]), "+v"(val[p][11]), "+v"(val[p][12]), "+v"(val[p][13]));
            }
            SBAR();
            STS(s2);
            SBAR();
            DRAIN();
            LAS const char* up = img + (4 * q4 + hh) * 64 + (G & 1) * 32 + p4 * 8;
            u32x4 us0 = trrd2(up, up + 1024), us1 = trrd2(up + 128, up + 1152);
            asm volatile("" : "+v"(us0), "+v"(us1) :: "memory");
            f32x16 kc = {}; u64 fu = 0;
            const unsigned fuo = WS_FU + ((ch >> 5) * 16 + (lane >> 2)) * 128 + (lane & 3) * 8;
            const unsigned uo = WS_U + ((ch >> 5) * 64 + lane) * 2176 + hf * 1024 + (ch & 31) * 16;
#pragma unroll
            for (int p = 0; p < 2; ++p) {
                                if (p == 1 && US_EARLY) { SBAR(); DRAIN(); if (lane == 0) __hip_atomic_store((GAS u64*)(ws + WS_FU + ((2 * sg + e) * 16 + art) * 128 + j * 8), tag, RLX); SBAR(); }
                if (p == 1 - US_EARLY) {
                    SBAR(); if (!US_EARLY) cnt_wait<2>(lds + L_SYNC + US_WORD, 4);
                    const unsigned o = WS_U + ((2 * sg + e) * 64 + art * 4 + j) * 2176 + lane * 16;
                    st16(wr, o, us0); st16(wr, o + 1024, us1);
                    SBAR();
                }
                auto put = [&](int cc, int k) { *(LAS u32x4*)(img + lane * 64 + ((cc ^ sw) << 4)) = val[p][k]; *(LAS u32x4*)(img + 4096 + lane * 64 + ((cc ^ sw) << 4)) = val[p][k + 1]; };
                put(0, 0); put(1, 2); put(2, 4);
                u32x4 bf[8], af[8];
#pragma unroll
                for (int ks = 0; ks < 8; ++ks) bf[ks] = trrd2(r0 + ks * 1024, r1 + ks * 1024);
                put(0, 6); put(1, 8); put(2, 10); put(3, 12);
                if (p == 1 && !US_EARLY) { SBAR(); DRAIN(); if (lane == 0) __hip_atomic_store((GAS u64*)(ws + WS_FU + ((2 * sg + e) * 16 + art) * 128 + j * 8), tag, RLX); SBAR(); }
#pragma unroll
                for (int ks = 0; ks < 8; ++ks) af[ks] = trrd2(r0 + ks * 1024, r1 + ks * 1024);
                if (p == 1 && US_EARLY) { SBAR(); cnt_wait<2>(lds + L_SYNC + 24, 4); fu = __hip_atomic_load((GAS u64*)(ws + fuo), RLX); asm volatile("" ::: "memory"); SBAR(); }
#pragma unroll
                for (int ks = 0; ks < 8; ++ks) kc = mfma32(bf[ks], af[ks], kc);
            }
            ST(3); STF(1, s1); STF(2, s2);
#pragma unroll
            for (int g = 0; g < 3; ++g) *(LAS f32x4*)(img + r32 * 96 + g * 32 + hh * 16) = f32x4{kc[4 * g], kc[4 * g + 1], kc[4 * g + 2], kc[4 * g + 3]};
            LAS char* sy = lds + L_SYNC + c * 8;
            pair_sync(sy, hf, 1);
            wait_flags<10>(ws, fuo, tag, fu); __builtin_amdgcn_fence(__ATOMIC_ACQUIRE, "wavefront");
            const u32x4 uv0 = ld16(wr, uo), uv1 = ld16(wr, uo + 512); SBAR();
            LAS char* kx = lds + L_KG + c * 16384 + 3072;
            {
                LAS const char* T = lds + L_KG + c * 16384;
                const int i = r32;
                float o[8];
                auto putk = [&](int sec) { *(LAS u32x4*)(kx + 64 + (sec * 32 + i) * 16) = u32x4{pk(o[0], o[1]), pk(o[2], o[3]), pk(o[4], o[5]), pk(o[6], o[7])}; };
                if (hf == 0) {
                    {
                        const int a0 = i * 96 + hh * 32, a1 = hh ? (i < 31 ? (i + 1) * 96 + 32 : 48) : i * 96 + 16;
                        const f32x4 v = *(LAS const f32x4*)(T + a0) + *(LAS const f32x4*)(T + 8192 + a0), n = *(LAS const f32x4*)(T + a1) + *(LAS const f32x4*)(T + 8192 + a1);
                        o[0] = v[0]; o[1] = hh ? v[0] : v[1]; o[2] = hh ? v[1] : v[2]; o[3] = hh ? v[2] : v[3];
                        o[4] = hh ? v[2] : n[0]; o[5] = hh ? v[2] : n[1]; o[6] = hh ? v[3] : n[2]; o[7] = hh ? n[0] : n[3];
                        if (lane == 0) o[0] += bflo(pDs);
                        putk(hh);
                    }
                    {
                        const int rw = 12 + 2 * hh, a0 = i * 96 + rw * 4, a1 = i < 31 ? a0 + 96 : (rw + 2) * 4;
                        const f32x2 v = *(LAS const f32x2*)(T + a0) + *(LAS const f32x2*)(T + 8192 + a0); const float n = *(LAS const float*)(T + a1) + *(LAS const float*)(T + 8192 + a1);
                        o[0] = o[1] = o[2] = v[0]; o[3] = o[4] = o[5] = v[1]; o[6] = o[7] = n;
                        putk(2 + hh);
                    }
                } else {
#pragma unroll
                    for (int k = 0; k < 2; ++k) {
                        const int rw = 16 + 2 * k + hh, a0 = i * 96 + rw * 4, a1 = i < 31 ? a0 + 96 : (rw + 1) * 4;
                        const float v = *(LAS const float*)(T + a0) + *(LAS const float*)(T + 8192 + a0), n = *(LAS const float*)(T + a1) + *(LAS const float*)(T + 8192 + a1);
                        o[0] = o[1] = o[2] = o[3] = v; o[4] = (i & 1) ? n : v; o[5] = o[6] = o[7] = n;
                        putk(4 + 2 * k + hh);
                    }
                    if (lane < 4) *(LAS u32x4*)(kx + lane * 16) = u32x4{0, 0, 0, 0};
                }
            }
            *(LAS u32x4*)(lds + L_U + (j * 2 + hh) * 1536 + r32 * 16) = u32x4{0, 0, 0, 0};
            const unsigned fyo = WS_FY + (hf * 32 + r32) * 128 + (xcd >> 1) * 32 + ((ch - (xcd >> 1) * 112) >> 5) * 8;
            unsigned fyp = WS_FY + frt * 128 + fct * 32 + j * 8; asm volatile("" : "+v"(fyp));
            *(LAS u32x4*)(lds + L_U + (j * 2) * 1536 + 512 + lane * 16) = uv0;
            *(LAS u32x4*)(lds + L_U + (j * 2 + 1) * 1536 + 512 + lane * 16) = uv1;
            mark_y(wr, frt, fct, j, lane);
            pair_sync(sy, hf, 2);
            ST(12);
            f32x16 ya0 = {}, ya1 = {}, yb0 = {}, yb1 = {};
            {
                LAS const char* ap = kx + (16 - 8 * hh - q4 + 4 * (pp & 1) + (pp >> 1)) * 8;
                LAS const char* bp = lds + L_U + (j * 2) * 1536 + (31 + r32 + hh) * 16;
                struct CF { u32x4 a0, a1, b0, b1, d0, d1; };
                auto rd = [&](LAS const char* a, LAS const char* bb) { CF f; f.a0 = trrd2(a + 32, a); f.b0 = *(LAS const u32x4*)(bb); f.b1 = *(LAS const u32x4*)(bb + 512);
                    f.d0 = *(LAS const u32x4*)(bb + 1536); f.d1 = *(LAS const u32x4*)(bb + 2048); f.a1 = trrd2(a + 2080, a + 2048); return f; };
                auto mm = [&](const CF& f) { ya0 = mfma32(f.a0, f.b0, ya0); ya1 = mfma32(f.a0, f.b1, ya1); yb0 = mfma32(f.a0, f.d0, yb0); yb1 = mfma32(f.a0, f.d1, yb1);
                    ya1 = mfma32(f.a1, f.b0, ya1); yb1 = mfma32(f.a1, f.d0, yb1); };
                ap = opq(ap - 32); bp = opq(bp - 480);
                CF f[2]; f[0] = rd(ap, bp + 480);
                SBAR(); PRIO(0);
#pragma unroll
                for (int g = 0; g < 16; ++g) {
                    if (g < CONV_G - 1) f[(g + 1) & 1] = rd(ap + (g + 1) * 128, bp + 480 - (g + 1) * 32);
                    SBAR(); if (g < CONV_G) mm(f[g & 1]); SBAR();
                    if (g == 4) asm volatile("s_waitcnt vmcnt(0)\n\ts_mov_b64 exec, 1\n\tglobal_store_dwordx2 %0, %1, %2 sc1\n\ts_mov_b64 exec, -1" :: "v"(fyp), "v"(tag), "s"(ws) : "memory");
                }
                PRIO(2);
            }
            const u64 fy = __hip_atomic_load((GAS u64*)(ws + fyo), RLX);
            SBAR();
            ST(13);
            {
                f32x16 own, oth; f32x4 s0, s1;
                if (hf == 0) {
                    own[0] = ya0[0]; oth[0] = ya1[0];
#pragma unroll
                    for (int r = 1; r < 16; ++r) { own[r] = ya0[r] + yb0[r - 1]; oth[r] = ya1[r] + yb1[r - 1]; }
                    s0 = f32x4{yb0[15], 0.f, 0.f, 0.f}; s1 = f32x4{yb1[15], 0.f, 0.f, 0.f};
                } else {
                    own[0] = own[1] = oth[0] = oth[1] = 0.f; own[2] = ya1[0]; oth[2] = ya0[0];
#pragma unroll
                    for (int r = 3; r < 16; ++r) { own[r] = ya1[r - 2] + yb1[r - 3]; oth[r] = ya0[r - 2] + yb0[r - 3]; }
                    s0 = f32x4{ya0[14] + yb0[13], ya0[15] + yb0[14], yb0[15], 0.f}; s1 = f32x4{ya1[14] + yb1[13], ya1[15] + yb1[14], yb1[15], 0.f};
                }
                LAS char* ex = lds + L_KG + c * 16384 + 8192;
#pragma unroll
                for (int g = 0; g < 4; ++g) *(LAS f32x4*)(ex + hf * 4096 + g * 1024 + lane * 16) = f32x4{oth[4 * g], oth[4 * g + 1], oth[4 * g + 2], oth[4 * g + 3]};
                const int blk = 2 * r32 + hh;
                LAS char* sp4 = lds + L_KG + c * 16384; LAS char* sp1 = sp4 + 7232;
                if (hf) { *(LAS f32x4*)(sp4 + (blk + 1) * 16) = s0; *(LAS f32x4*)(sp4 + (blk + 65) * 16) = s1; if (lane == 0) *(LAS f32x4*)sp4 = f32x4{0.f, 0.f, 0.f, 0.f}; }
                else { *(LAS float*)(sp1 + (blk + 1) * 4) = s0[0]; *(LAS float*)(sp1 + (blk + 65) * 4) = s1[0]; if (lane == 0) *(LAS float*)sp1 = 0.f; }
                pair_sync(sy, hf, 3);
#pragma unroll
                for (int g = 0; g < 4; ++g) { const f32x4 v = *(LAS const f32x4*)(ex + (hf ^ 1) * 4096 + g * 1024 + lane * 16);
#pragma unroll
                    for (int i = 0; i < 4; ++i) own[4 * g + i] += v[i]; }
                const f32x4 sa = *(LAS const f32x4*)(sp4 + (hf * 64 + blk) * 16);
                own[0] += sa[0] + *(LAS const float*)(sp1 + (hf * 64 + blk) * 4); own[1] += sa[1]; own[2] += sa[2];
                LAS char* yt = lds + L_U + j * 3072;
                *(LAS u32x4*)(yt + blk * 32) = u32x4{pk(own[0], own[1]), pk(own[2], own[3]), pk(own[4], own[5]), pk(own[6], own[7])};
                *(LAS u32x4*)(yt + blk * 32 + 16) = u32x4{pk(own[8], own[9]), pk(own[10], own[11]), pk(own[12], own[13]), pk(own[14], own[15])};
                const unsigned yo = WS_Y + ((hf * 32 + (lane >> 2)) * 450 + ch) * 64 + p4 * 16;
                u32x4 ya = *(LAS const u32x4*)(yt + lane * 16), yb = *(LAS const u32x4*)(yt + 1024 + lane * 16);
#pragma unroll
                for (int i = 0; i < 4; ++i) { ya[i] = min(ya[i], 0xfffffffeu); yb[i] = min(yb[i], 0xfffffffeu); }
                wait_flags<8>(ws, fyo, tag, fy);
                st16(wr, yo, ya); st16(wr, yo + 16 * 450 * 64, yb);
            }
        }
    }
    asm volatile("" : "+s"(role));
    RELANE();
    if (role == 2) {
        {
            const unsigned wl = ((bi & 3) * 4 + j) * 112 + lane; int m0s;
            asm volatile("s_mov_b32 %0, m0\n\ts_mov_b32 m0, %4\n\ts_nop 0\n\tglobal_load_lds_dword %1, %3\n\ts_mov_b32 exec_hi, 0xffff\n\tglobal_load_lds_dword %2, %3\n\ts_mov_b32 exec_hi, -1\n\ts_mov_b32 m0, %0"
                         : "=&s"(m0s) : "v"(__umul24(wl >> 2, 2688u) + (3 * e + (wl & 3)) * 128), "v"(__umul24((wl >> 2) + 16, 2688u) + (3 * e + (wl & 3)) * 128), "s"(wqkv), "s"(lds0 + L_SYNC + 512) : "memory");
        }
        tag = __hip_atomic_load((GAS u64*)(ws + WS_E), RLX) + TAG_C;
        const rsrc_t wr = mk_rsrc(ws);
        LAS char* img = lds + L_KG + j * 8192;
        u32x4 wv[4], gv[7];
        if (hh) *(LAS u32x4*)(img + 3072 + lane * 16) = u32x4{0, 0, 0, 0};
#pragma unroll
        for (int k = 0; k < 7; ++k) gv[k] = *(LAS const u32x4*)(img + 4096 + k * 128 + (lane & 7) * 16);
        cnt_wait<6>(lds + L_SYNC + 20, 4);
        const int row0 = art * 128 + e * 64 + j * 16;
        float z[2];
#pragma unroll 1
        for (int it = 0; it < 2; ++it) {
            float a = 0.f;
#pragma unroll
            for (int k = 0; k < 7; ++k) {
                const u32x4 xv = *(const u32x4*)(x + (row0 + it * 8 + (lane >> 3)) * D + k * 64 + (lane & 7) * 8);
#pragma unroll
                for (int i = 0; i < 4; ++i) { const unsigned xi = xv[i], gi = gv[k][i]; a = __builtin_amdgcn_fdot2_f32_bf16(__builtin_bit_cast(bf16x2, xi), __builtin_bit_cast(bf16x2, gi), a, false); }
            }
            a += __shfl_xor(a, 1); a += __shfl_xor(a, 2); a += __shfl_xor(a, 4);
            z[1] = __shfl(a, (lane & 7) * 8); if (it == 0) z[0] = z[1];
        }
        float gate = 1.f / (1.f + __expf(-(lane & 8 ? z[1] : z[0])));
#pragma unroll
        for (int i = 0; i < 4; ++i) {
            LAS const char* p = img + (16 * (i & 1) + 8 * hh + q4) * 128 + (i >> 1) * 64 + (G & 1) * 32 + (((p4 & 1) << 1) | (p4 >> 1)) * 8;
            wv[i] = trrd2(p, p + 512);
        }
        asm volatile("" : "+v"(wv[0]), "+v"(wv[1]), "+v"(wv[2]), "+v"(wv[3]), "+v"(gate));
        cnt_wait<6>(lds + L_SYNC + X_WORD, 4);
#pragma unroll
        for (int i = 0; i < 4; ++i) st16(wr, WS_W + (((2 * e + c) * 4 + 2 * hf + (i >> 1)) * 32 + art * 2 + (i & 1)) * 1024 + lane * 16, wv[i]);
        if (lane < 16) __hip_atomic_store((GAS float*)(ws + WS_G) + row0 + lane, gate, RLX);
        mark_y(wr, frt, fct, j, lane);
        DRAIN();
        ST2(15);
        if (lane < 2) __hip_atomic_store((GAS u64*)(ws + (lane ? WS_FY + frt * 128 + fct * 32 + j * 8 : WS_FW + ((art * 2 + e) * 4 + j) * 8)), tag, RLX);
    }
    ST(16);
    RELANE();
    const rsrc_t wr = mk_rsrc(ws);
    u32x4 wf[32]; f32x16 yt; float gt;
    {
        cnt_wait(lds + L_SYNC + 24, 4);
        int old = 0;
        if (lane == 0) old = __hip_atomic_fetch_add((LAS int*)(lds + L_SYNC + 60), 1, __ATOMIC_RELAXED, __HIP_MEMORY_SCOPE_WORKGROUP);
        const bool lastw = __builtin_amdgcn_readfirstlane(old) == 3;
        const unsigned tl = (unsigned)tag, th = (unsigned)(tag >> 32), fh = WS_FH + ((art * 2 + e) * 4 + j) * 128 + 64;
        if (lastw && lane == 0) __hip_atomic_store((GAS u64*)(ws + WS_F2 + (xcd * 32 + bi) * 8), tag, RLX);
        if (hj == 6 && lane == 0) __hip_atomic_store((GAS u64*)(ws + fh), tag, RLX);
        if (hj == 7) __builtin_amdgcn_s_sleep(64);
#pragma unroll 1
        for (unsigned n = 0; n < SPIN_CAP; ++n) {
            asm volatile("" ::: "memory");
            const u32x4 f = ld16(wr, WS_FW + lane * 16);
            if (__all(f[0] == tl && f[1] == th && f[2] == tl && f[3] == th)) break;
            __builtin_amdgcn_s_sleep(32);
        }
#pragma unroll
        for (int i = 0; i < 32; ++i) {
            wf[i] = ld16(wr, WS_W + (fct * 4 + j) * 32768 + i * 1024 + lane * 16);
            if (i & 1) { SBAR(); __builtin_amdgcn_s_sleep(WO_PACE); SBAR(); }
        }
        if (hj == 7) { wait_flags<16>(ws, fh, tag); __builtin_amdgcn_s_sleep(40); }
        ST(17);
        const unsigned yo = WS_Y + (frt * 450 + fct * 112) * 64 + j * 2048 + lane * 16;
        u32x4 y0, y1;
#pragma unroll 1
        for (unsigned n = 0; n < SPIN_CAP; ++n) {
            asm volatile("" ::: "memory");
            y0 = ld16(wr, yo); y1 = ld16(wr, j < 3 ? yo + 1024 : WS_END);
            if (__all(max(max(max(y0[0], y0[1]), max(y0[2], y0[3])), max(max(y1[0], y1[1]), max(y1[2], y1[3]))) != ~0u)) break;
            __builtin_amdgcn_s_sleep(Y_RETRY);
        }
        gt = __hip_atomic_load((GAS float*)(ws + WS_G) + frt * 32 + r32, RLX);
        LAS char* ys = lds + L_Y + j * 2048;
        *(LAS u32x4*)(ys + lane * 16) = y0; *(LAS u32x4*)(ys + 1024 + lane * 16) = y1;
#pragma unroll
        for (int i = 0; i < 4; ++i) {
            const u32x2 y = trrd(ys + (16 * (i >> 1) + 8 * hh + 4 * (i & 1) + q4) * 64 + (G & 1) * 32 + p4 * 8);
            yt[4 * i] = (1.f - gt) * bflo(y[0]); yt[4 * i + 1] = (1.f - gt) * bfhi(y[0]); yt[4 * i + 2] = (1.f - gt) * bflo(y[1]); yt[4 * i + 3] = (1.f - gt) * bfhi(y[1]);
        }
        ST(19);
    }
    u16* op = out + (frt * 32 + (lane >> 2)) * D + fct * 112 + j * 32 + p4 * 8;
    LAS char* ow = opq(lds + L_Y + j * 2048 + r32 * 64 + ((hh ^ ((r32 >> 1) & 3)) << 4));
    LAS char* ow2 = opq(lds + L_Y + j * 2048 + r32 * 64 + (((2 + hh) ^ ((r32 >> 1) & 3)) << 4));
    LAS const char* orr = opq(lds + L_Y + j * 2048 + (lane >> 2) * 64 + ((p4 ^ ((lane >> 3) & 3)) << 4));
    LAS const char* of0 = opq(lds + L_OS + lane * 16);
    asm volatile("" : "+v"(yt), "+v"(gt), "+v"(op));
    if (b == 0 && j == 3) {
#pragma unroll 1
        for (int k = 0; k < 4; ++k) wait_flags<8>(ws, WS_F2 + (k * 64 + lane) * 8, tag);
        if (lane == 0) __hip_atomic_store((GAS u64*)(ws + WS_E), tag, RLX);
    }
    PRIO(3);
    BARRIER();
    ST(22);
    {
        f32x16 c0 = {};
        u32x4 of[8];
#pragma unroll
        for (int i = 0; i < 38; ++i) {
            if (i < 32) of[i & 7] = *(LAS const u32x4*)(of0 + i * 1024);
            SBAR();
            if (i >= 6) c0 = mfma32(wf[i - 6], of[(i - 6) & 7], c0);
            SBAR();
        }
#pragma unroll
        for (int i = 0; i < 16; ++i) c0[i] = __builtin_fmaf(gt, c0[i], yt[i]);
        *(LAS u32x4*)ow = u32x4{pk(c0[0], c0[1]), pk(c0[2], c0[3]), pk(c0[4], c0[5]), pk(c0[6], c0[7])};
        *(LAS u32x4*)ow2 = u32x4{pk(c0[8], c0[9]), pk(c0[10], c0[11]), pk(c0[12], c0[13]), pk(c0[14], c0[15])};
        const u32x4 o0 = *(LAS const u32x4*)orr, o1 = *(LAS const u32x4*)(orr + 1024);
        if (j < 3 || p4 < 2) { *(u32x4*)op = o0; *(u32x4*)(op + 16 * D) = o1; }
    }
    ST(23);
    ST_FLUSH();
}

extern "C" void kernel_launch(void* const* d_in, const int* in_sizes, int n_in, void* d_out, int out_size, void* d_ws, size_t ws_size, hipStream_t stream) {
    mk<<<256, 512, 0, stream>>>((const u16*)d_in[0], (const u16*)d_in[1], (const u16*)d_in[2], (const u16*)d_in[4], (const u16*)d_in[5], (const u16*)d_in[6], (const u16*)d_in[7],
                                (const u16*)d_in[8], (const u16*)d_in[9], (const u16*)d_in[10], (const u16*)d_in[11], (const u16*)d_in[12], (char*)d_ws, (u16*)d_out);
}
```
